```python
import torch

def _softmax(x, axis=-1):
    return torch.softmax(x, dim=axis)
def _gelu(x):
    return torch.nn.functional.gelu(x, approximate="tanh")
def _silu(x):
    return torch.nn.functional.silu(x)
def _sigmoid(x):
    return torch.sigmoid(x)
def _layernorm(x, axis=-1, eps=1e-5):
    m = x.mean(dim=axis, keepdim=True)
    v = x.var(dim=axis, keepdim=True, unbiased=False)
    return (x - m) / torch.sqrt(v + eps)
def _scatter_add(tgt, idx, src, axis=0):
    return tgt.clone().index_add_(axis, idx, src)
def _segment_sum(src, seg, n):
    out = torch.zeros((n,) + src.shape[1:], dtype=src.dtype)
    return out.index_add_(0, seg, src)
def _pad(x, pw):
    flat = tuple(v for pair in reversed(pw) for v in pair)
    return torch.nn.functional.pad(x, flat)

kernel_name = 'sequential_preLN__gqa_block_cached__dense_activation_squared_relu__decode'

def setup_inputs():

    _w = (lambda a: (lambda t: t.to(torch.bfloat16) if t.is_floating_point() else t)(torch.as_tensor(a)))
    def _A_setup():
        B, Sq, D = 1, 1, 576
        Hq, Hkv, Sk, Dh = 32, 16, 65536, 128
        assert Hq % Hkv == 0, (Hq, Hkv)
        return {
            "x": _w((lambda *s: torch.randn(*s, dtype=torch.bfloat16) * 0.05)(B, Sq, D)),
            "k_cache": _w((lambda *s: torch.randn(*s, dtype=torch.bfloat16) * 3.2)(B, Hkv, Sk, Dh)),
            "v_cache": _w((lambda *s: torch.randn(*s, dtype=torch.bfloat16) * 0.4)(B, Hkv, Sk, Dh)),
            "wq": _w((lambda *s: torch.randn(*s, dtype=torch.bfloat16) * 3.2)(D, Hq * Dh) * (D ** -0.5)),
            "wo": _w((lambda *s: torch.randn(*s, dtype=torch.bfloat16) * 0.4)(Hq * Dh, D) * ((Hq * Dh) ** -0.5)),
        }
    _A_in = _A_setup()

    def _B_setup():
        B, S, D, F = 1, 1, 576, 4608
        return {
            "x": _w((lambda *s: torch.randn(*s, dtype=torch.bfloat16) * 0.05)(B, S, D)),
            "w1": _w((lambda *s: torch.randn(*s, dtype=torch.bfloat16) * 0.05)(D, F)),
            "b1": _w((lambda *s: torch.randn(*s, dtype=torch.bfloat16) * 0.05)(F)),
            "w2": _w((lambda *s: torch.randn(*s, dtype=torch.bfloat16) * 0.05)(F, D)),
            "b2": _w((lambda *s: torch.randn(*s, dtype=torch.bfloat16) * 0.05)(D)),
        }
    _B_in = _B_setup()

    return {
        "A_x": _A_in["x"],
        "A_k_cache": _A_in["k_cache"],
        "A_v_cache": _A_in["v_cache"],
        "A_wq": _A_in["wq"],
        "A_wo": _A_in["wo"],
        "B_x": _B_in["x"],
        "B_w1": _B_in["w1"],
        "B_b1": _B_in["b1"],
        "B_w2": _B_in["w2"],
        "B_b2": _B_in["b2"],
    }

def reference(A_x, A_k_cache, A_v_cache, A_wq, A_wo, B_x, B_w1, B_b1, B_w2, B_b2):
    def _A_ref(x, k_cache, v_cache, wq, wo):
        B, Sq, D = x.shape
        Hq, Dh = 32, 128
        Hkv = k_cache.shape[1]
        rep = Hq // Hkv
        q = (lambda x, *d: x.permute(*d))((x @ wq).reshape(B, Sq, Hq, Dh), 0, 2, 1, 3)
        k = (lambda x, n, axis: torch.repeat_interleave(x, n, dim=axis))(k_cache, rep, axis=1)
        v = (lambda x, n, axis: torch.repeat_interleave(x, n, dim=axis))(v_cache, rep, axis=1)
        scores = torch.einsum("bhqd,bhkd->bhqk", q, k) * (Dh ** -0.5)
        p = _softmax(scores, axis=-1)
        o = torch.einsum("bhqk,bhkd->bhqd", p, v)
        return (lambda x, *d: x.permute(*d))(o, 0, 2, 1, 3).reshape(B, Sq, Hq * Dh) @ wo

    def _B_ref(x, w1, b1, w2, b2):
        h = torch.einsum("bsd,df->bsf", x, w1) + b1
        zero = torch.zeros_like(h)
        r = torch.where(h > zero, h, zero)
        h = r * r
        out = torch.einsum("bsf,fd->bsd", h, w2) + b2
        return out

    x = A_x
    h = x + _A_ref(_layernorm(x, axis=-1), A_k_cache, A_v_cache, A_wq, A_wo)
    h2 = h + _B_ref(_layernorm(h, axis=-1), B_w1, B_b1, B_w2, B_b2)
    return h2
```

```cpp
#include <hip/hip_runtime.h>
#include <stdint.h>

typedef unsigned short u16;
typedef unsigned long long u64;
typedef unsigned v4u __attribute__((ext_vector_type(4)));
typedef unsigned v2u __attribute__((ext_vector_type(2)));
typedef float v4f __attribute__((ext_vector_type(4)));
typedef float v2f __attribute__((ext_vector_type(2)));
typedef short v8s __attribute__((ext_vector_type(8)));
typedef __bf16 v2bf __attribute__((ext_vector_type(2)));
#define GAS __attribute__((address_space(1)))
#define LAS __attribute__((address_space(3)))

#define DBG_STAMP 0
#define STEAL 1

constexpr int D = 576, FF = 4608, NQ = 4096;
constexpr int NTHR = 576;
constexpr unsigned WS_E = 0, WS_QG = 4096;
constexpr unsigned WS_ML = WS_QG + 16384;
constexpr unsigned WS_P = WS_ML + 8192;
constexpr unsigned WS_Y = WS_P + 32 * 2048 * 4, WS_Z = WS_Y + 32 * 576 * 4;
constexpr unsigned WS_END = WS_Z + 144 * 576 * 4;
constexpr unsigned WS_DBG = 8u << 20;
#define AUXQ 16
#define AUXT 16
#define RING 3
#define TAU 0
#define TZERO 350
#define PRE2 1
#define QPS 8
constexpr int L_S = 0;
constexpr int L_QP = 32768;
constexpr int L_Q = L_QP + 576;
constexpr int L_ML = L_Q + 512;
constexpr int L_CT = L_ML + 128;
constexpr int L_CT2 = L_CT + 16;
constexpr int L_F = L_CT2 + 16;
constexpr int L_OACC = L_F + 128;
constexpr int L_R1 = L_OACC + 8192;
constexpr int L_R2 = L_R1 + 6144;
constexpr int L_R4 = L_R2 + 1152;
constexpr int L_H = L_R4 + 1152;
constexpr int L_USED = L_H + 2304;
constexpr int LDS_BYTES = 84 * 1024;
static_assert(L_USED <= LDS_BYTES, "lds");
static_assert(L_QP % 16 == 0 && L_Q % 16 == 0 && L_ML % 16 == 0 && L_F % 16 == 0 && L_OACC % 16 == 0 && L_R1 % 16 == 0 && L_R2 % 16 == 0 && L_R4 % 16 == 0 && L_H % 16 == 0, "align");

constexpr int AUX_NT = 2, AUX_SC1 = 16;
constexpr unsigned SPIN_CAP = 400000;
constexpr float LOG2E = 1.4426950408889634f;

__device__ __forceinline__ float asf(unsigned u) { return __builtin_bit_cast(float, u); }
__device__ __forceinline__ unsigned asu(float f) { return __builtin_bit_cast(unsigned, f); }
__device__ __forceinline__ float bflo(unsigned u) { return asf(u << 16); }
__device__ __forceinline__ float bfhi(unsigned u) { return asf(u & 0xffff0000u); }
__device__ __forceinline__ unsigned cvtpk(float lo, float hi) { v2f f = {lo, hi}; return __builtin_bit_cast(unsigned, __builtin_convertvector(f, v2bf)); }
__device__ __forceinline__ float rbf(float f) { return asf(cvtpk(f, f) << 16); }
__device__ __forceinline__ float dot2(unsigned a, unsigned b, float c) { return __builtin_amdgcn_fdot2_f32_bf16(__builtin_bit_cast(v2bf, a), __builtin_bit_cast(v2bf, b), c, false); }
template <int CTRL> __device__ __forceinline__ float dpp(float x) { return __builtin_bit_cast(float, __builtin_amdgcn_mov_dpp(__builtin_bit_cast(int, x), CTRL, 0xf, 0xf, true)); }
template <int CTRL> __device__ __forceinline__ unsigned dppu(unsigned x) { return (unsigned)__builtin_amdgcn_mov_dpp((int)x, CTRL, 0xf, 0xf, true); }
constexpr int XOR1 = 0xB1, XOR2 = 0x4E, XOR3 = 0x1B, XOR7 = 0x141, XOR15 = 0x140, XOR8 = 0x128, ROR4 = 0x124;
__device__ __forceinline__ float x16sum(float x) { auto s = __builtin_amdgcn_permlane16_swap(asu(x), asu(x), false, false); return asf(s[0]) + asf(s[1]); }
__device__ __forceinline__ float x32sum(float x) { auto s = __builtin_amdgcn_permlane32_swap(asu(x), asu(x), false, false); return asf(s[0]) + asf(s[1]); }
__device__ __forceinline__ float x16tr(float a, float b) { auto s = __builtin_amdgcn_permlane16_swap(asu(a), asu(b), false, false); return asf(s[0]) + asf(s[1]); }
__device__ __forceinline__ float x32tr(float a, float b) { auto s = __builtin_amdgcn_permlane32_swap(asu(a), asu(b), false, false); return asf(s[0]) + asf(s[1]); }
__device__ __forceinline__ float x16max(float x) { auto s = __builtin_amdgcn_permlane16_swap(asu(x), asu(x), false, false); return fmaxf(asf(s[0]), asf(s[1])); }
__device__ __forceinline__ float x32max(float x) { auto s = __builtin_amdgcn_permlane32_swap(asu(x), asu(x), false, false); return fmaxf(asf(s[0]), asf(s[1])); }
__device__ __forceinline__ float row16sum(float x) { x += dpp<XOR1>(x); x += dpp<XOR2>(x); x += dpp<XOR7>(x); x += dpp<XOR15>(x); return x; }
__device__ __forceinline__ float row16max(float x) { x = fmaxf(x, dpp<XOR1>(x)); x = fmaxf(x, dpp<XOR2>(x)); x = fmaxf(x, dpp<XOR7>(x)); x = fmaxf(x, dpp<XOR15>(x)); return x; }
__device__ __forceinline__ float wavesum(float x) { return x32sum(x16sum(row16sum(x))); }
__device__ __forceinline__ v4f mfma(v4u a, v4u b, v4f c) { return __builtin_amdgcn_mfma_f32_16x16x32_bf16(__builtin_bit_cast(v8s, a), __builtin_bit_cast(v8s, b), c, 0, 0, 0); }
__device__ __forceinline__ v4u ldnt(const void* p) { return __builtin_nontemporal_load((const GAS v4u*)p); }
__device__ __forceinline__ v4u poll128(__amdgpu_buffer_rsrc_t r, unsigned off, unsigned soff) {
  asm volatile("" : "+v"(off) :: "memory");
  return __builtin_amdgcn_raw_buffer_load_b128(r, off, soff, AUXT);
}
__device__ __forceinline__ float tr8(const float (&a)[8], int lane) {
  float c[4];
#pragma unroll
  for (int k = 0; k < 4; ++k) { const float t1 = a[k] + dpp<XOR8>(a[k]), t2 = a[k + 4] + dpp<XOR8>(a[k + 4]); c[k] = (lane & 8) ? t2 : t1; }
  return x32tr(x16tr(c[0], c[2]), x16tr(c[1], c[3]));
}
#define SB() __builtin_amdgcn_sched_barrier(0)
#define W_PERM() do { if (is_mlp) { const int src_ = (r * 4 + gq) * 4; _Pragma("unroll") for (int p = 0; p < 4; ++p) _Pragma("unroll") for (int k = 0; k < 4; ++k) W[p][k] = (unsigned)__builtin_amdgcn_ds_bpermute(src_, (int)W[p][k]); } \
    _Pragma("unroll") for (int p = 0; p < 4; ++p) { v4u lo, hi; \
    _Pragma("unroll") for (int k = 0; k < 4; ++k) { lo[k] = __builtin_amdgcn_perm(W[2 * p + 1][k], W[2 * p][k], 0x05040100u); hi[k] = __builtin_amdgcn_perm(W[2 * p + 1][k], W[2 * p][k], 0x07060302u); } \
    W[2 * p] = lo; W[2 * p + 1] = hi; asm volatile("" : "+v"(W[2 * p]), "+v"(W[2 * p + 1])); } } while (0)
#define WAIT_VM(n) asm volatile("s_waitcnt vmcnt(" #n ")" ::: "memory")
#define BAR() do { asm volatile("s_waitcnt lgkmcnt(0)" ::: "memory"); __builtin_amdgcn_s_barrier(); asm volatile("" ::: "memory"); } while (0)
__device__ __forceinline__ unsigned enc(float f, unsigned tg) { return ((asu(f) + 0x800u) & 0xfffff000u) | tg; }
__device__ __forceinline__ v4u mark4(v4f v, unsigned tg) { v4u r = {enc(v[0], tg), enc(v[1], tg), enc(v[2], tg), enc(v[3], tg)}; return r; }
__device__ __forceinline__ unsigned bad4(v4u v, unsigned tg) { return ((v[0] ^ tg) | (v[1] ^ tg) | (v[2] ^ tg) | (v[3] ^ tg)) & 0xfffu; }
__device__ __forceinline__ v4f dec4(v4u v) { v4u m = v & 0xfffff000u; return __builtin_bit_cast(v4f, m); }

#define STAMP(i) do { } while (0)

__global__ void __launch_bounds__(NTHR, 3)
fused_decode(const u16* __restrict__ x, const u16* __restrict__ Kc, const u16* __restrict__ Vc, const u16* __restrict__ wq,
             const u16* __restrict__ wo, const u16* __restrict__ w1, const u16* __restrict__ b1, const u16* __restrict__ w2,
             const u16* __restrict__ b2, u16* __restrict__ out, unsigned char* ws) {
  __shared__ __attribute__((aligned(16))) unsigned char lds_raw[LDS_BYTES];
  LAS unsigned char* lds = (LAS unsigned char*)lds_raw;
  const int tid = threadIdx.x, lane = tid & 63;
  const int wave = __builtin_amdgcn_readfirstlane(tid >> 6);
  const int b = blockIdx.x;
  const int g = b >> 4, s = b & 15;
  const int xcd = b & 7, li = b >> 3;
  const int cb = xcd * 32 + li;
  const __amdgpu_buffer_rsrc_t rW = __builtin_amdgcn_make_buffer_rsrc((void*)ws, 0, WS_END, 0x00020000);
  STAMP(0);
  const unsigned tstart = (unsigned)__builtin_amdgcn_s_memrealtime();
  const int qrow = 64 * wave + (lane >> 1);
  const v4u wq0 = ldnt(wq + (size_t)qrow * NQ + 16 * cb + 8 * (lane & 1));
  const v4u wq1 = ldnt(wq + (size_t)(qrow + 32) * NQ + 16 * cb + 8 * (lane & 1));
  const v4u xa = ldnt(x + 8 * lane);
  const unsigned xb = __builtin_nontemporal_load((const GAS u16*)x + 512 + lane);
  const unsigned xr0 = __builtin_nontemporal_load((const GAS u16*)x + qrow), xr1 = __builtin_nontemporal_load((const GAS u16*)x + qrow + 32);
  unsigned ev = __builtin_amdgcn_raw_buffer_load_b32(rW, 0, WS_E, AUX_SC1 | AUX_NT);
  if (tid < 10) *(LAS v4u*)(lds + L_ML + tid * 16) = v4u{0u, 0u, 0u, 0u};
  SB();
  BAR();
  SB();
  const __amdgpu_buffer_rsrc_t rK = __builtin_amdgcn_make_buffer_rsrc((void*)(Kc + ((size_t)b << 19)), 0, wave < 8 ? 1 << 20 : 0, 0x00020000);
  const __amdgpu_buffer_rsrc_t rV = __builtin_amdgcn_make_buffer_rsrc((void*)(Vc + ((size_t)g << 23)), 0, 1 << 24, 0x00020000);
  const unsigned wbase = (unsigned)wave * 4096u;
  const int gq = lane >> 4, r = lane & 15;
  const unsigned voffK = (unsigned)((r >> 2) * 256 + gq * 64 + (r & 3) * 16), voffV = (unsigned)lane * 16;
#define LDK(tile_, j_) __builtin_amdgcn_raw_buffer_load_b128(rK, voffK + (j_) * 1024, wbase + (unsigned)(tile_) * 32768u, AUX_NT)
#define LDV(off_, j_) __builtin_amdgcn_raw_buffer_load_b128(rV, voffV + (j_) * 1024, (off_), AUX_NT)
  const unsigned vbase = ((unsigned)s << 20) + wbase;
  v4u k0[4], k1[4];
#pragma unroll
  for (int j = 0; j < 4; ++j) k0[j] = LDK(0, j);
#pragma unroll
  for (int j = 0; j < 4; ++j) k1[j] = LDK(1, j);
  SB();
  {
    const unsigned ones = 0x3f803f80u;
    const float xe = bflo(xb);
    const float mean = wavesum(dot2(xa[0], ones, dot2(xa[1], ones, xe)) + dot2(xa[2], ones, dot2(xa[3], ones, 0.f))) * (1.0f / D);
    float d = xe - mean, sq = d * d, sq2 = 0.f;
#pragma unroll
    for (int k = 0; k < 4; ++k) { const float d0 = bflo(xa[k]) - mean, d1 = bfhi(xa[k]) - mean; sq += d0 * d0; sq2 += d1 * d1; }
    const float var = wavesum(sq + sq2) * (1.0f / D);
    const float mb = rbf(mean), sd = rbf(__builtin_amdgcn_sqrtf(rbf(rbf(var) + 1e-5f)));
    const float n0 = rbf(bflo(xr0) - mb) / sd, n1 = rbf(bflo(xr1) - mb) / sd;
    const unsigned xp = cvtpk(n0, n1);
    float a[8];
#pragma unroll
    for (int k = 0; k < 4; ++k) {
      a[2 * k] = dot2(__builtin_amdgcn_perm(wq1[k], wq0[k], 0x05040100u), xp, 0.f);
      a[2 * k + 1] = dot2(__builtin_amdgcn_perm(wq1[k], wq0[k], 0x07060302u), xp, 0.f);
    }
    float t = tr8(a, lane);
    t += dpp<XOR2>(t);
    t += dpp<XOR3>(dpp<XOR7>(t));
    if ((lane & 6) == 0) ((LAS float*)(lds + L_QP))[wave * 16 + 8 * (lane & 1) + ((lane >> 3) & 1) * 4 + ((lane >> 4) & 1) * 2 + (lane >> 5)] = t;
  }
  STAMP(1);
  asm volatile("s_waitcnt lgkmcnt(0)" ::: "memory");
  if (lane == 0) __hip_atomic_fetch_add((LAS unsigned*)(lds + L_CT2), 1u, __ATOMIC_RELAXED, __HIP_MEMORY_SCOPE_WORKGROUP);
  volatile LAS unsigned* ctl2 = (volatile LAS unsigned*)(lds + L_CT2);
  const unsigned epoch = __builtin_amdgcn_readfirstlane(ev) + 1u;
  const unsigned tg = epoch & 0xfffu;
  const int rho = (s + g) & 15;
  const bool is_mlp = rho < 9, is_wo = (rho >= 9 && rho < 15);
  const int lm = (s >> 3) * 9 + rho;
  const int mj = xcd * 18 + lm;
  const int witem = xcd * 12 + (s >> 3) * 6 + (rho - 9), wh = witem / 3, wcg = witem - wh * 3;
  const u16* wpa; const u16* wpb; size_t sta, stb;
  if (is_mlp) {
    wpa = w1 + (size_t)(4 * (16 * wave + (lane >> 2))) * FF + 32 * mj + 8 * (lane & 3); sta = FF;
    wpb = w2 + (size_t)(32 * mj + 4 * (lane >> 3)) * D + 64 * wave + 8 * (lane & 7); stb = D;
  } else if (is_wo && wave < 8) {
    const int ch = (lane & 31) < 24 ? (lane & 31) : 23;
    wpa = wo + (size_t)(128 * wh + 16 * wave + 8 * (lane >> 5)) * D + 192 * wcg + 8 * ch; sta = D;
    wpb = wpa + 4 * D; stb = D;
  } else {
    wpa = wo; wpb = wo; sta = 0; stb = 0;
  }
  v4u W[8];
  const int mjc = is_mlp ? mj : 0;
  const bool is_fin = is_mlp && (lm < 2 || (lm == 2 && xcd < 2));
  const int fj = lm < 2 ? xcd * 2 + lm : 16 + xcd;
  v2u b1v = __builtin_nontemporal_load((const GAS v2u*)(b1 + 32 * mjc + 4 * (lane >> 3)));
  unsigned b2v = __builtin_nontemporal_load((const GAS u16*)b2 + (is_fin ? fj : 0) * 32 + (lane & 31));
  v2u xq = __builtin_nontemporal_load((const GAS v2u*)(x + 4 * (16 * wave + r)));

  v4u mlq = {0u, 0u, 0u, 0u};
  if (wave < 8) {
    v4u k2[4], k3[4], R[4];
    WAIT_VM(5);
    W[0] = ldnt(wpa); k2[0] = LDK(2, 0);
    WAIT_VM(2);
    W[1] = ldnt(wpa + sta); k2[1] = LDK(2, 1);
    WAIT_VM(2);
    W[2] = ldnt(wpa + 2 * sta); k2[2] = LDK(2, 2);
    WAIT_VM(2);
    W[3] = ldnt(wpa + 3 * sta); k2[3] = LDK(2, 3);
    WAIT_VM(2);
    W[4] = ldnt(wpb); k3[0] = LDK(3, 0);
    WAIT_VM(2);
    W[5] = ldnt(wpb + stb); k3[1] = LDK(3, 1);
    WAIT_VM(2);
    W[6] = ldnt(wpb + 2 * stb); k3[2] = LDK(3, 2);
    WAIT_VM(2);
    W[7] = ldnt(wpb + 3 * stb); k3[3] = LDK(3, 3);
    WAIT_VM(2);
    R[0] = LDK(4, 0);
    WAIT_VM(2);
    R[1] = LDK(4, 1);
    WAIT_VM(2);
    R[2] = LDK(4, 2);
    STAMP(5);
    _Pragma("unroll 1") for (unsigned it = 0; it < 64u * SPIN_CAP; ++it) { if (__builtin_amdgcn_readfirstlane(ctl2[1])) break; __builtin_amdgcn_s_sleep(1); }
    asm volatile("" ::: "memory");
    STAMP(2);
    const v4u qf = *(const LAS v4u*)(lds + L_Q + ((r >> 2) & 1) * 256 + (4 * gq + (r & 3)) * 16);
    const v4u zero4 = {0u, 0u, 0u, 0u};
    const v4u B0 = (r < 8) ? qf : zero4, B1 = (r < 8) ? zero4 : qf;
    LAS float* Sw = (LAS float*)(lds + L_S + wave * 4096);
    const int sidx = gq * 8 + ((r >> 2) & 1) * 4 + (r & 2) + (r >> 3);
    float mx = -INFINITY;
    const v4f z4f = {0.f, 0.f, 0.f, 0.f};
#define K_POST(i_) do { \
      const float keep = (r & 2) ? ((r & 1) ? Db[3] : Db[2]) : ((r & 1) ? Da[1] : Da[0]); \
      const float send = (r & 2) ? ((r & 1) ? Da[3] : Da[2]) : ((r & 1) ? Db[1] : Db[0]); \
      float y = keep + dpp<XOR2>(send); y += dpp<XOR1>(y); \
      const float sc = rbf(rbf(y) * 0.08838834764831845f); \
      mx = fmaxf(mx, sc); \
      if ((r & 1) == 0) Sw[(i_) * 32 + sidx] = sc; } while (0)
    unsigned tgt = (tstart + (unsigned)TZERO) * 16u + 4u * (unsigned)TAU;
#define TSTAMP() do { } while (0)
#define PACE() do { TSTAMP(); if (TAU) { tgt += (unsigned)TAU; while ((int)((unsigned)__builtin_amdgcn_s_memrealtime() * 16u - tgt) < 0) __builtin_amdgcn_s_sleep(1); } } while (0)
#define STEP4(OP0, OP1, OP2, OP3, LC, LN) do { \
      OP0; SB(); R[3] = LC(3); SB(); OP1; SB(); R[0] = LN(0); SB(); OP2; SB(); R[1] = LN(1); SB(); OP3; SB(); R[2] = LN(2); SB(); } while (0)
#define K_TILE(t_, LC, LN) do { v4f Da, Db; \
      STEP4(Da = mfma(R[0], B0, z4f), Da = mfma(R[1], B1, Da), Db = mfma(R[2], B0, z4f), Db = mfma(R[3], B1, Db), LC, LN); \
      K_POST(t_); PACE(); } while (0)
#define K_PRE(t_, X) do { v4f Da = mfma(X[0], B0, z4f); Da = mfma(X[1], B1, Da); v4f Db = mfma(X[2], B0, z4f); Db = mfma(X[3], B1, Db); K_POST(t_); } while (0)
    {
#define LC_(j) LDK(4, j)
#define LN_(j) LDK(5, j)
    K_TILE(4, LC_, LN_);
#undef LC_
#undef LN_
    }
    K_PRE(0, k0); K_PRE(1, k1); K_PRE(2, k2); K_PRE(3, k3);
#pragma unroll 1
    for (int t = 5; t < 31; ++t) {
#define LC_(j) LDK(t, j)
#define LN_(j) LDK(t + 1, j)
      K_TILE(t, LC_, LN_);
#undef LC_
#undef LN_
    }
    {
#define LC_(j) LDK(31, j)
#define LN_(j) LDV(vbase, j)
    K_TILE(31, LC_, LN_);
#undef LC_
#undef LN_
    }
    STAMP(3);
    asm volatile("" ::: "memory");
    mx = fmaxf(mx, dpp<XOR1>(mx)); mx = fmaxf(mx, dpp<XOR2>(mx)); mx = fmaxf(mx, dpp<XOR8>(mx)); mx = x32max(x16max(mx));
    const float m0 = __builtin_bit_cast(float, __builtin_amdgcn_readlane((int)asu(mx), 0)), m1 = __builtin_bit_cast(float, __builtin_amdgcn_readlane((int)asu(mx), 4));
    {
      const float mh = (lane & 1) ? m1 : m0;
      float l = 0.f;
      v4f sv[4];
#pragma unroll
      for (int k = 0; k < 4; ++k) sv[k] = *(const LAS v4f*)((LAS unsigned char*)Sw + (lane + 64 * k) * 16);
#pragma unroll
      for (int k = 0; k < 4; ++k) {
        v4u pk;
#pragma unroll
        for (int e = 0; e < 4; ++e) { const float p = __builtin_amdgcn_exp2f((sv[k][e] - mh) * LOG2E); l += p; pk[e] = cvtpk(p, p); }
        *(LAS v4u*)((LAS unsigned char*)Sw + (lane + 64 * k) * 16) = pk;
      }
      l += dpp<XOR2>(l); l += dpp<ROR4>(l); l += dpp<XOR8>(l); l = x32sum(x16sum(l));
      const float l0 = __builtin_bit_cast(float, __builtin_amdgcn_readlane((int)asu(l), 0)), l1 = __builtin_bit_cast(float, __builtin_amdgcn_readlane((int)asu(l), 1));
      if (lane == 0) *(LAS v4f*)(lds + L_ML + wave * 16) = v4f{m0, l0, m1, l1};
    }
    asm volatile("" ::: "memory");
    unsigned msk[4];
#pragma unroll
    for (int w = 0; w < 4; ++w) msk[w] = ((r >> 2) == w) ? ((r & 1) ? 0xffff0000u : 0x0000ffffu) : 0u;
    const unsigned pql = (unsigned)(gq * 8 + ((r >> 1) & 1) * 4) * 4;
    const LAS unsigned char* pbase = (const LAS unsigned char*)Sw + pql;
    v4f acc = z4f;
    v4u pq = *(const LAS v4u*)(pbase);
#define AOP(l_) v4u{pq[l_] & msk[0], pq[l_] & msk[1], pq[l_] & msk[2], pq[l_] & msk[3]}
#define LCV_(j) LDV(cur__, j)
#define LNV_(j) LDV(nxt__, j)
#define V_TILE(cur_, nxt_, pn_) do { \
      const v4u A0 = AOP(0), A1 = AOP(1), A2 = AOP(2), A3 = AOP(3); \
      pq = *(const LAS v4u*)(pbase + (pn_) * 128); SB(); \
      const unsigned cur__ = (cur_), nxt__ = (nxt_); (void)cur__; \
      STEP4(acc = mfma(A0, R[0], acc), acc = mfma(A1, R[1], acc), acc = mfma(A2, R[2], acc), acc = mfma(A3, R[3], acc), LCV_, LNV_); } while (0)
#define VOWN(n_) (vbase + (unsigned)(n_) * 32768u)
    V_TILE(VOWN(0), VOWN(1), 1); PACE();
    W_PERM();
#pragma unroll 1
    for (int n = 1; n < 31; ++n) { V_TILE(VOWN(n), VOWN(n + 1), n + 1); PACE(); }
    V_TILE(VOWN(31), 0x7f000000u, 0);
    STAMP(4);
    LAS float* oa = (LAS float*)(lds + L_OACC + wave * 1024);
    *(LAS v2f*)(oa + 8 * r + 2 * gq) = v2f{acc[0], acc[1]};
    *(LAS v2f*)(oa + 128 + 8 * r + 2 * gq) = v2f{acc[2], acc[3]};
  } else {
    _Pragma("unroll 1") for (unsigned it = 0; it < 64u * SPIN_CAP; ++it) { if (__builtin_amdgcn_readfirstlane(ctl2[0]) >= 9u) break; }
    asm volatile("" ::: "memory");
    const unsigned qt = epoch << 16;
    {
      float v = 0.f;
#pragma unroll
      for (int w = 0; w < 9; ++w) v += ((const LAS float*)(lds + L_QP))[w * 16 + r];
      const unsigned gr = qt | (cvtpk(v, v) & 0xffffu);
      const v4u g4 = {dppu<0x00>(gr), dppu<0x55>(gr), dppu<0xAA>(gr), dppu<0xFF>(gr)};
      if ((lane & 51) == 0) __builtin_amdgcn_raw_buffer_store_b128(g4, rW, WS_QG + (unsigned)(16 * cb + lane) * 4, 0, AUXQ);
    }
    STAMP(2);
    const unsigned o0 = WS_QG + (unsigned)g * 1024u + (unsigned)lane * 16u;
    const v4u rs4 = {(unsigned)(size_t)ws, (unsigned)((size_t)ws >> 32), WS_END, 0x00020000u};
    v4u pa, pc, pe;
#define QISSUE(A_) asm volatile("buffer_load_dwordx4 %0, %1, %2, 0 offen sc1" : "=&v"(A_) : "v"(o0), "s"(rs4) : "memory")
#define QSTAGE(A_) { \
      asm volatile("s_waitcnt vmcnt(2)" : "+v"(A_) :: "memory"); \
      *(LAS v2u*)(lds + L_Q + lane * 8) = v2u{__builtin_amdgcn_perm(A_[1], A_[0], 0x05040100u), __builtin_amdgcn_perm(A_[3], A_[2], 0x05040100u)}; \
      if (__all((((A_[0] ^ qt) | (A_[1] ^ qt) | (A_[2] ^ qt) | (A_[3] ^ qt)) >> 16) == 0u)) break; \
      QISSUE(A_); }
    QISSUE(pa);
    __builtin_amdgcn_s_sleep(QPS);
    QISSUE(pc);
    __builtin_amdgcn_s_sleep(QPS);
    QISSUE(pe);
    _Pragma("unroll 1") for (unsigned it = 0; it < SPIN_CAP; ++it) {
      QSTAGE(pa);
      QSTAGE(pc);
      QSTAGE(pe);
    }
    asm volatile("s_waitcnt lgkmcnt(0)" ::: "memory");
    if (lane == 0) ctl2[1] = 1u;
    asm volatile("s_waitcnt vmcnt(0)" : "+v"(pa), "+v"(pc), "+v"(pe) :: "memory");
    STAMP(3);
#pragma unroll
    for (int i = 0; i < 4; ++i) { W[i] = ldnt(wpa + i * sta); W[4 + i] = ldnt(wpb + i * stb); }
    W_PERM();
    v4f ml;
    _Pragma("unroll 1") for (unsigned it = 0; it < 64u * SPIN_CAP; ++it) {
      ml = *(const volatile LAS v4f*)(lds + L_ML + (lane & 7) * 16);
      if (__all(asu(ml[1]) != 0u && asu(ml[3]) != 0u)) break;
      __builtin_amdgcn_s_sleep(16);
    }
    STAMP(4);
    float M0 = ml[0], M1 = ml[2];
    M0 = fmaxf(M0, dpp<XOR1>(M0)); M0 = fmaxf(M0, dpp<XOR2>(M0)); M0 = fmaxf(M0, dpp<XOR7>(M0));
    M1 = fmaxf(M1, dpp<XOR1>(M1)); M1 = fmaxf(M1, dpp<XOR2>(M1)); M1 = fmaxf(M1, dpp<XOR7>(M1));
    const float f0 = __builtin_amdgcn_exp2f((ml[0] - M0) * LOG2E), f1 = __builtin_amdgcn_exp2f((ml[2] - M1) * LOG2E);
    float L0 = ml[1] * f0, L1 = ml[3] * f1;
    L0 += dpp<XOR1>(L0); L0 += dpp<XOR2>(L0); L0 += dpp<XOR7>(L0);
    L1 += dpp<XOR1>(L1); L1 += dpp<XOR2>(L1); L1 += dpp<XOR7>(L1);
    if (lane < 8) *(LAS v2f*)(lds + L_F + lane * 8) = v2f{f0, f1};
    mlq = (lane & 1) ? mark4(v4f{M1, L1, 0.f, 0.f}, tg) : mark4(v4f{M0, L0, 0.f, 0.f}, tg);
    STAMP(5);
  }
  asm volatile("" : "+v"(b1v), "+v"(b2v), "+v"(xq));
  BAR();
  if (wave < 8) {
    const int h = wave >> 2;
    const float f = ((const LAS float*)(lds + L_F))[(lane >> 3) * 2 + h];
    v4f o = f * *(const LAS v4f*)(lds + L_OACC + (lane >> 3) * 1024 + wave * 128 + (lane & 7) * 16);
#pragma unroll
    for (int e = 0; e < 4; ++e) { float t = o[e]; t += dpp<XOR8>(t); o[e] = x32sum(x16sum(t)); }
    if (lane < 8) __builtin_amdgcn_raw_buffer_store_b128(mark4(o, tg), rW, WS_P + (unsigned)((2 * g + h) * 16 + s) * 512 + (wave & 3) * 128 + lane * 16, 0, AUXT);
  } else if (lane < 2) __builtin_amdgcn_raw_buffer_store_b128(mlq, rW, WS_ML + (unsigned)((2 * g + lane) * 16 + s) * 16, 0, AUXT);
  STAMP(6);
  if (is_wo) {
    if (wave < 8) {
      v4u pv, mv;
      const unsigned off = WS_P + (unsigned)(wh * 16 + (lane >> 2)) * 512 + wave * 64 + (lane & 3) * 16;
      const unsigned offm = WS_ML + (unsigned)(wh * 16 + (lane >> 2)) * 16;
      _Pragma("unroll 1") for (unsigned it = 0; it < SPIN_CAP; ++it) {
        pv = poll128(rW, off, 0); mv = poll128(rW, offm, 0);
        if (__all((bad4(pv, tg) | bad4(mv, tg)) == 0u)) break;
      }
      STAMP(14);
      const float ms = asf(mv[0] & 0xfffff000u);
      float Mx = fmaxf(ms, dpp<ROR4>(ms)); Mx = fmaxf(Mx, dpp<XOR8>(Mx)); Mx = x32max(x16max(Mx));
      const float wsf = __builtin_amdgcn_exp2f((ms - Mx) * LOG2E);
      float Ls = wsf * asf(mv[1] & 0xfffff000u);
      Ls += dpp<ROR4>(Ls); Ls += dpp<XOR8>(Ls); Ls = x32sum(x16sum(Ls));
      const v4f o = wsf * dec4(pv);
      float t = x32tr(x16tr(o[0], o[2]), x16tr(o[1], o[3]));
      t += dpp<ROR4>(t); t += dpp<XOR8>(t);
      t *= __builtin_amdgcn_rcpf(Ls);
      const auto sw = __builtin_amdgcn_permlane32_swap(asu(t), asu(t), false, false);
      const unsigned pr = cvtpk(asf(sw[0]), asf(sw[1]));
      const auto s2 = __builtin_amdgcn_permlane16_swap(pr, pr, false, false);
      const unsigned p01 = s2[0], p23 = s2[1];
      const bool hi = lane >= 32;
      const unsigned c0 = dppu<0x00>(p01), c1 = dppu<0x55>(p01), c2 = dppu<0xAA>(p01), c3 = dppu<0xFF>(p01);
      const unsigned d0 = dppu<0x00>(p23), d1 = dppu<0x55>(p23), d2 = dppu<0xAA>(p23), d3 = dppu<0xFF>(p23);
      const unsigned a0 = hi ? c2 : c0, a1 = hi ? d2 : d0, a2 = hi ? c3 : c1, a3 = hi ? d3 : d1;
      float acc8[8];
#pragma unroll
      for (int k = 0; k < 4; ++k) {
        acc8[2 * k] = dot2(W[6][k], a3, dot2(W[4][k], a2, dot2(W[2][k], a1, dot2(W[0][k], a0, 0.f))));
        acc8[2 * k + 1] = dot2(W[7][k], a3, dot2(W[5][k], a2, dot2(W[3][k], a1, dot2(W[1][k], a0, 0.f))));
      }
      const v4f y4 = {x32tr(acc8[0], acc8[4]), x32tr(acc8[1], acc8[5]), x32tr(acc8[2], acc8[6]), x32tr(acc8[3], acc8[7])};
      if ((lane & 31) < 24) *(LAS v4f*)(lds + L_R1 + wave * 768 + (lane & 31) * 32 + (lane >> 5) * 16) = y4;
    }
    BAR();
    if (wave < 6) {
      v4f y = *(const LAS v4f*)(lds + L_R1 + (lane >> 3) * 768 + wave * 128 + (lane & 7) * 16);
#pragma unroll
      for (int e = 0; e < 4; ++e) { float t = y[e]; t += dpp<XOR8>(t); y[e] = x32sum(x16sum(t)); }
      if (lane < 8) __builtin_amdgcn_raw_buffer_store_b128(mark4(y, tg), rW, WS_Y + (unsigned)(wh * 576 + wcg * 192) * 4 + wave * 128 + lane * 16, 0, AUXT);
    }
    STAMP(8);
  } else if (is_mlp) {
    v4f hq;
    {
      const unsigned off = WS_Y + (unsigned)(gq * 8 * 576 + (16 * wave + r) * 4) * 4;
      v4u yv[8];
#pragma unroll
      for (int i = 0; i < 8; ++i) yv[i] = v4u{0u, 0u, 0u, 0u};
      unsigned need = 0xffu;
      _Pragma("unroll 1") for (unsigned it = 0; it < SPIN_CAP && need; ++it) {
#pragma unroll
        for (int i = 0; i < 8; ++i) if (need & (1u << i)) yv[i] = poll128(rW, off, i * 2304);
#pragma unroll
        for (int i = 0; i < 8; ++i) if (need & (1u << i)) { if (__all(bad4(yv[i], tg) == 0u)) need &= ~(1u << i); }
        need = __builtin_amdgcn_readfirstlane(need);
      }
      STAMP(14);
      v4f y = ((dec4(yv[0]) + dec4(yv[1])) + (dec4(yv[2]) + dec4(yv[3]))) + ((dec4(yv[4]) + dec4(yv[5])) + (dec4(yv[6]) + dec4(yv[7])));
#pragma unroll
      for (int e = 0; e < 4; ++e) y[e] = x32sum(x16sum(y[e]));
      hq[0] = rbf(bflo(xq[0]) + rbf(y[0])); hq[1] = rbf(bfhi(xq[0]) + rbf(y[1])); hq[2] = rbf(bflo(xq[1]) + rbf(y[2])); hq[3] = rbf(bfhi(xq[1]) + rbf(y[3]));
    }
    const float mw = row16sum((hq[0] + hq[1]) + (hq[2] + hq[3])) * (1.0f / 64.0f);
    const float e0 = hq[0] - mw, e1 = hq[1] - mw, e2 = hq[2] - mw, e3 = hq[3] - mw;
    const float m2w = row16sum((e0 * e0 + e1 * e1) + (e2 * e2 + e3 * e3));
    if (lane == 0) *(LAS v2f*)(lds + L_R1 + wave * 8) = v2f{mw, m2w};
    if (lane < 16) *(LAS v4f*)(lds + L_H + (16 * wave + lane) * 16) = hq;
    BAR();
    STAMP(11);
    {
      const v2f st = *(const LAS v2f*)(lds + L_R1 + (r < 9 ? r : 0) * 8);
      const float mean2 = row16sum(r < 9 ? st[0] : 0.f) * (1.0f / 9.0f);
      const float dm = st[0] - mean2;
      const float var2 = row16sum(r < 9 ? st[1] + 64.0f * dm * dm : 0.f) * (1.0f / D);
      const float mb2 = rbf(mean2), sd2 = rbf(__builtin_amdgcn_sqrtf(rbf(rbf(var2) + 1e-5f)));
      const float rs = __builtin_amdgcn_rcpf(sd2);
      const unsigned n01 = cvtpk(rbf(hq[0] - mb2) * rs, rbf(hq[1] - mb2) * rs), n23 = cvtpk(rbf(hq[2] - mb2) * rs, rbf(hq[3] - mb2) * rs);
      float a[8];
#pragma unroll
      for (int k = 0; k < 4; ++k) { a[2 * k] = dot2(W[2][k], n23, dot2(W[0][k], n01, 0.f)); a[2 * k + 1] = dot2(W[3][k], n23, dot2(W[1][k], n01, 0.f)); }
#pragma unroll
      for (int k = 0; k < 4; ++k) { const float t1 = a[k] + dpp<XOR8>(a[k]), t2 = a[k + 4] + dpp<XOR8>(a[k + 4]); a[k] = (lane & 8) ? t2 : t1; }
#pragma unroll
      for (int k = 0; k < 2; ++k) { const float t1 = a[k] + dpp<XOR7>(a[k]), t2 = a[k + 2] + dpp<XOR7>(a[k + 2]); a[k] = (lane & 4) ? t2 : t1; }
      { const float t1 = a[0] + dpp<XOR2>(a[0]), t2 = a[1] + dpp<XOR2>(a[1]); a[0] = (lane & 2) ? t2 : t1; }
      a[0] += dpp<XOR1>(a[0]);
      if ((lane & 1) == 0) ((LAS float*)(lds + L_R2))[wave * 32 + (lane >> 1)] = a[0];
    }
    STAMP(12);
    BAR();
    STAMP(13);
    {
      v4f hs = {0.f, 0.f, 0.f, 0.f};
#pragma unroll
      for (int w = 0; w < 9; ++w) hs += *(const LAS v4f*)(lds + L_R2 + w * 128 + (lane >> 3) * 16);
      float t0 = rbf(rbf(hs[0]) + bflo(b1v[0])), t1 = rbf(rbf(hs[1]) + bfhi(b1v[0])), t2 = rbf(rbf(hs[2]) + bflo(b1v[1])), t3 = rbf(rbf(hs[3]) + bfhi(b1v[1]));
      t0 = t0 > 0.f ? t0 : 0.f; t1 = t1 > 0.f ? t1 : 0.f; t2 = t2 > 0.f ? t2 : 0.f; t3 = t3 > 0.f ? t3 : 0.f;
      const unsigned u01 = cvtpk(t0 * t0, t1 * t1), u23 = cvtpk(t2 * t2, t3 * t3);
      float a[8];
#pragma unroll
      for (int k = 0; k < 4; ++k) { a[2 * k] = dot2(W[6][k], u23, dot2(W[4][k], u01, 0.f)); a[2 * k + 1] = dot2(W[7][k], u23, dot2(W[5][k], u01, 0.f)); }
      v4f z;
#pragma unroll
      for (int e = 0; e < 4; ++e) { float t = x32tr(a[e], a[e + 4]); t += dpp<XOR8>(t); z[e] = x16sum(t); }
      if ((lane & 24) == 0) __builtin_amdgcn_raw_buffer_store_b128(mark4(z, tg), rW, WS_Z + (unsigned)mj * 2304 + wave * 256 + (lane & 7) * 32 + (lane >> 5) * 16, 0, AUXT);
    }
    STAMP(8);
    if (is_fin) {
      const unsigned off = WS_Z + (unsigned)(tid >> 3) * 2304 + fj * 128 + (tid & 7) * 16;
      v4u z0, z1;
      _Pragma("unroll 1") for (unsigned it = 0; it < SPIN_CAP; ++it) {
        z0 = poll128(rW, off, 0); z1 = poll128(rW, off, 72 * 2304);
        if (__all((bad4(z0, tg) | bad4(z1, tg)) == 0u)) break;
      }
      STAMP(9);
      const v4f zf = dec4(z0) + dec4(z1);
      float t = x32tr(x16tr(zf[0], zf[2]), x16tr(zf[1], zf[3]));
      t += dpp<XOR8>(t);
      if ((lane & 8) == 0) ((LAS float*)(lds + L_R4))[wave * 32 + (lane & 7) * 4 + ((lane >> 4) & 1) * 2 + (lane >> 5)] = t;
      BAR();
      if (tid < 32) {
        float v = 0.f;
#pragma unroll
        for (int w = 0; w < 9; ++w) v += ((const LAS float*)(lds + L_R4))[w * 32 + tid];
        const float hres = ((const LAS float*)(lds + L_H))[fj * 32 + tid];
        const float o = hres + rbf(rbf(v) + bflo(b2v));
        ((GAS u16*)out)[fj * 32 + tid] = (u16)cvtpk(o, o);
        if (fj == 0 && tid == 0) __builtin_amdgcn_raw_buffer_store_b32(epoch, rW, 0, WS_E, AUX_SC1);
      }
      STAMP(10);
    }
  }
}

extern "C" void kernel_launch(void* const* d_in, const int* in_sizes, int n_in, void* d_out, int out_size, void* d_ws, size_t ws_size, hipStream_t stream) {
  hipLaunchKernelGGL(fused_decode, dim3(256), dim3(NTHR), 0, stream,
                     (const u16*)d_in[0], (const u16*)d_in[1], (const u16*)d_in[2], (const u16*)d_in[3], (const u16*)d_in[4],
                     (const u16*)d_in[6], (const u16*)d_in[7], (const u16*)d_in[8], (const u16*)d_in[9], (u16*)d_out, (unsigned char*)d_ws);
}
```

```cpp
#define AUXK 2
#define AUXV 2
#include <hip/hip_runtime.h>
#include <stdint.h>

typedef unsigned short u16;
typedef unsigned long long u64;
typedef unsigned v4u __attribute__((ext_vector_type(4)));
typedef unsigned v2u __attribute__((ext_vector_type(2)));
typedef float v4f __attribute__((ext_vector_type(4)));
typedef float v2f __attribute__((ext_vector_type(2)));
typedef short v8s __attribute__((ext_vector_type(8)));
typedef __bf16 v2bf __attribute__((ext_vector_type(2)));
#define GAS __attribute__((address_space(1)))
#define LAS __attribute__((address_space(3)))

#define DBG_STAMP 0
#define STEAL 1

constexpr int D = 576, FF = 4608, NQ = 4096;
constexpr int NTHR = 576;
constexpr unsigned WS_E = 0, WS_QG = 4096;
constexpr unsigned WS_ML = WS_QG + 16384;
constexpr unsigned WS_P = WS_ML + 8192;
constexpr unsigned WS_Y = WS_P + 32 * 2048 * 4, WS_Z = WS_Y + 32 * 576 * 4;
constexpr unsigned WS_END = WS_Z + 144 * 576 * 4;
constexpr unsigned WS_DBG = 8u << 20;
#define AUXQ 16
#define AUXT 16
#define RING 3
#define TAU 0
#define TZERO 350
#define PRE2 1
#define QPS 8
constexpr int L_S = 0;
constexpr int L_QP = 32768;
constexpr int L_Q = L_QP + 576;
constexpr int L_ML = L_Q + 512;
constexpr int L_CT = L_ML + 128;
constexpr int L_CT2 = L_CT + 16;
constexpr int L_F = L_CT2 + 16;
constexpr int L_OACC = L_F + 128;
constexpr int L_R1 = L_OACC + 8192;
constexpr int L_R2 = L_R1 + 6144;
constexpr int L_R4 = L_R2 + 1152;
constexpr int L_H = L_R4 + 1152;
constexpr int L_USED = L_H + 2304;
constexpr int LDS_BYTES = 84 * 1024;
static_assert(L_USED <= LDS_BYTES, "lds");
static_assert(L_QP % 16 == 0 && L_Q % 16 == 0 && L_ML % 16 == 0 && L_F % 16 == 0 && L_OACC % 16 == 0 && L_R1 % 16 == 0 && L_R2 % 16 == 0 && L_R4 % 16 == 0 && L_H % 16 == 0, "align");

constexpr int AUX_NT = 2, AUX_SC1 = 16;
constexpr unsigned SPIN_CAP = 400000;
constexpr float LOG2E = 1.4426950408889634f;

__device__ __forceinline__ float asf(unsigned u) { return __builtin_bit_cast(float, u); }
__device__ __forceinline__ unsigned asu(float f) { return __builtin_bit_cast(unsigned, f); }
__device__ __forceinline__ float bflo(unsigned u) { return asf(u << 16); }
__device__ __forceinline__ float bfhi(unsigned u) { return asf(u & 0xffff0000u); }
__device__ __forceinline__ unsigned cvtpk(float lo, float hi) { v2f f = {lo, hi}; return __builtin_bit_cast(unsigned, __builtin_convertvector(f, v2bf)); }
__device__ __forceinline__ float rbf(float f) { return asf(cvtpk(f, f) << 16); }
__device__ __forceinline__ float dot2(unsigned a, unsigned b, float c) { return __builtin_amdgcn_fdot2_f32_bf16(__builtin_bit_cast(v2bf, a), __builtin_bit_cast(v2bf, b), c, false); }
template <int CTRL> __device__ __forceinline__ float dpp(float x) { return __builtin_bit_cast(float, __builtin_amdgcn_mov_dpp(__builtin_bit_cast(int, x), CTRL, 0xf, 0xf, true)); }
template <int CTRL> __device__ __forceinline__ unsigned dppu(unsigned x) { return (unsigned)__builtin_amdgcn_mov_dpp((int)x, CTRL, 0xf, 0xf, true); }
constexpr int XOR1 = 0xB1, XOR2 = 0x4E, XOR3 = 0x1B, XOR7 = 0x141, XOR15 = 0x140, XOR8 = 0x128, ROR4 = 0x124;
__device__ __forceinline__ float x16sum(float x) { auto s = __builtin_amdgcn_permlane16_swap(asu(x), asu(x), false, false); return asf(s[0]) + asf(s[1]); }
__device__ __forceinline__ float x32sum(float x) { auto s = __builtin_amdgcn_permlane32_swap(asu(x), asu(x), false, false); return asf(s[0]) + asf(s[1]); }
__device__ __forceinline__ float x16tr(float a, float b) { auto s = __builtin_amdgcn_permlane16_swap(asu(a), asu(b), false, false); return asf(s[0]) + asf(s[1]); }
__device__ __forceinline__ float x32tr(float a, float b) { auto s = __builtin_amdgcn_permlane32_swap(asu(a), asu(b), false, false); return asf(s[0]) + asf(s[1]); }
__device__ __forceinline__ float x16max(float x) { auto s = __builtin_amdgcn_permlane16_swap(asu(x), asu(x), false, false); return fmaxf(asf(s[0]), asf(s[1])); }
__device__ __forceinline__ float x32max(float x) { auto s = __builtin_amdgcn_permlane32_swap(asu(x), asu(x), false, false); return fmaxf(asf(s[0]), asf(s[1])); }
__device__ __forceinline__ float row16sum(float x) { x += dpp<XOR1>(x); x += dpp<XOR2>(x); x += dpp<XOR7>(x); x += dpp<XOR15>(x); return x; }
__device__ __forceinline__ float row16max(float x) { x = fmaxf(x, dpp<XOR1>(x)); x = fmaxf(x, dpp<XOR2>(x)); x = fmaxf(x, dpp<XOR7>(x)); x = fmaxf(x, dpp<XOR15>(x)); return x; }
__device__ __forceinline__ float wavesum(float x) { return x32sum(x16sum(row16sum(x))); }
__device__ __forceinline__ v4f mfma(v4u a, v4u b, v4f c) { return __builtin_amdgcn_mfma_f32_16x16x32_bf16(__builtin_bit_cast(v8s, a), __builtin_bit_cast(v8s, b), c, 0, 0, 0); }
__device__ __forceinline__ v4u ldnt(const void* p) { return __builtin_nontemporal_load((const GAS v4u*)p); }
__device__ __forceinline__ v4u poll128(__amdgpu_buffer_rsrc_t r, unsigned off, unsigned soff) {
  asm volatile("" : "+v"(off) :: "memory");
  return __builtin_amdgcn_raw_buffer_load_b128(r, off, soff, AUXT);
}
__device__ __forceinline__ float tr8(const float (&a)[8], int lane) {
  float c[4];
#pragma unroll
  for (int k = 0; k < 4; ++k) { const float t1 = a[k] + dpp<XOR8>(a[k]), t2 = a[k + 4] + dpp<XOR8>(a[k + 4]); c[k] = (lane & 8) ? t2 : t1; }
  return x32tr(x16tr(c[0], c[2]), x16tr(c[1], c[3]));
}
#define SB() __builtin_amdgcn_sched_barrier(0)
#define W_PERM() do { if (is_mlp) { const int src_ = (r * 4 + gq) * 4; _Pragma("unroll") for (int p = 0; p < 4; ++p) _Pragma("unroll") for (int k = 0; k < 4; ++k) W[p][k] = (unsigned)__builtin_amdgcn_ds_bpermute(src_, (int)W[p][k]); } \
    _Pragma("unroll") for (int p = 0; p < 4; ++p) { v4u lo, hi; \
    _Pragma("unroll") for (int k = 0; k < 4; ++k) { lo[k] = __builtin_amdgcn_perm(W[2 * p + 1][k], W[2 * p][k], 0x05040100u); hi[k] = __builtin_amdgcn_perm(W[2 * p + 1][k], W[2 * p][k], 0x07060302u); } \
    W[2 * p] = lo; W[2 * p + 1] = hi; asm volatile("" : "+v"(W[2 * p]), "+v"(W[2 * p + 1])); } } while (0)
#define WAIT_VM(n) asm volatile("s_waitcnt vmcnt(" #n ")" ::: "memory")
#define BAR() do { asm volatile("s_waitcnt lgkmcnt(0)" ::: "memory"); __builtin_amdgcn_s_barrier(); asm volatile("" ::: "memory"); } while (0)
__device__ __forceinline__ unsigned enc(float f, unsigned tg) { return ((asu(f) + 0x800u) & 0xfffff000u) | tg; }
__device__ __forceinline__ v4u mark4(v4f v, unsigned tg) { v4u r = {enc(v[0], tg), enc(v[1], tg), enc(v[2], tg), enc(v[3], tg)}; return r; }
__device__ __forceinline__ unsigned bad4(v4u v, unsigned tg) { return ((v[0] ^ tg) | (v[1] ^ tg) | (v[2] ^ tg) | (v[3] ^ tg)) & 0xfffu; }
__device__ __forceinline__ v4f dec4(v4u v) { v4u m = v & 0xfffff000u; return __builtin_bit_cast(v4f, m); }

#define STAMP(i) do { } while (0)

__global__ void __launch_bounds__(NTHR, 3)
fused_decode(const u16* __restrict__ x, const u16* __restrict__ Kc, const u16* __restrict__ Vc, const u16* __restrict__ wq,
             const u16* __restrict__ wo, const u16* __restrict__ w1, const u16* __restrict__ b1, const u16* __restrict__ w2,
             const u16* __restrict__ b2, u16* __restrict__ out, unsigned char* ws) {
  __shared__ __attribute__((aligned(16))) unsigned char lds_raw[LDS_BYTES];
  LAS unsigned char* lds = (LAS unsigned char*)lds_raw;
  const int tid = threadIdx.x, lane = tid & 63;
  const int wave = __builtin_amdgcn_readfirstlane(tid >> 6);
  const int b = blockIdx.x;
  const int g = b >> 4, s = b & 15;
  const int xcd = b & 7, li = b >> 3;
  const int cb = xcd * 32 + li;
  const __amdgpu_buffer_rsrc_t rW = __builtin_amdgcn_make_buffer_rsrc((void*)ws, 0, WS_END, 0x00020000);
  STAMP(0);
  const unsigned tstart = (unsigned)__builtin_amdgcn_s_memrealtime();
  const int qrow = 64 * wave + (lane >> 1);
  const v4u wq0 = ldnt(wq + (size_t)qrow * NQ + 16 * cb + 8 * (lane & 1));
  const v4u wq1 = ldnt(wq + (size_t)(qrow + 32) * NQ + 16 * cb + 8 * (lane & 1));
  const v4u xa = ldnt(x + 8 * lane);
  const unsigned xb = __builtin_nontemporal_load((const GAS u16*)x + 512 + lane);
  const unsigned xr0 = __builtin_nontemporal_load((const GAS u16*)x + qrow), xr1 = __builtin_nontemporal_load((const GAS u16*)x + qrow + 32);
  unsigned ev = __builtin_amdgcn_raw_buffer_load_b32(rW, 0, WS_E, AUX_SC1 | AUX_NT);
  if (tid < 10) *(LAS v4u*)(lds + L_ML + tid * 16) = v4u{0u, 0u, 0u, 0u};
  SB();
  BAR();
  SB();
  const __amdgpu_buffer_rsrc_t rK = __builtin_amdgcn_make_buffer_rsrc((void*)(Kc + ((size_t)b << 19)), 0, wave < 8 ? 1 << 20 : 0, 0x00020000);
  const __amdgpu_buffer_rsrc_t rV = __builtin_amdgcn_make_buffer_rsrc((void*)(Vc + ((size_t)g << 23)), 0, 1 << 24, 0x00020000);
  const unsigned wbase = (unsigned)wave * 4096u;
  const int gq = lane >> 4, r = lane & 15;
  const unsigned voffK = (unsigned)((r >> 2) * 256 + gq * 64 + (r & 3) * 16), voffV = (unsigned)lane * 16;
#define LDK(tile_, j_) __builtin_amdgcn_raw_buffer_load_b128(rK, voffK + (j_) * 1024, wbase + (unsigned)(tile_) * 32768u, AUXK)
#define LDV(off_, j_) __builtin_amdgcn_raw_buffer_load_b128(rV, voffV + (j_) * 1024, (off_), AUXV)
  const unsigned vbase = ((unsigned)s << 20) + wbase;
  v4u k0[4], k1[4];
#pragma unroll
  for (int j = 0; j < 4; ++j) k0[j] = LDK(0, j);
#pragma unroll
  for (int j = 0; j < 4; ++j) k1[j] = LDK(1, j);
  SB();
  {
    const unsigned ones = 0x3f803f80u;
    const float xe = bflo(xb);
    const float mean = wavesum(dot2(xa[0], ones, dot2(xa[1], ones, xe)) + dot2(xa[2], ones, dot2(xa[3], ones, 0.f))) * (1.0f / D);
    float d = xe - mean, sq = d * d, sq2 = 0.f;
#pragma unroll
    for (int k = 0; k < 4; ++k) { const float d0 = bflo(xa[k]) - mean, d1 = bfhi(xa[k]) - mean; sq += d0 * d0; sq2 += d1 * d1; }
    const float var = wavesum(sq + sq2) * (1.0f / D);
    const float mb = rbf(mean), sd = rbf(__builtin_amdgcn_sqrtf(rbf(rbf(var) + 1e-5f)));
    const float n0 = rbf(bflo(xr0) - mb) / sd, n1 = rbf(bflo(xr1) - mb) / sd;
    const unsigned xp = cvtpk(n0, n1);
    float a[8];
#pragma unroll
    for (int k = 0; k < 4; ++k) {
      a[2 * k] = dot2(__builtin_amdgcn_perm(wq1[k], wq0[k], 0x05040100u), xp, 0.f);
      a[2 * k + 1] = dot2(__builtin_amdgcn_perm(wq1[k], wq0[k], 0x07060302u), xp, 0.f);
    }
    float t = tr8(a, lane);
    t += dpp<XOR2>(t);
    t += dpp<XOR3>(dpp<XOR7>(t));
    if ((lane & 6) == 0) ((LAS float*)(lds + L_QP))[wave * 16 + 8 * (lane & 1) + ((lane >> 3) & 1) * 4 + ((lane >> 4) & 1) * 2 + (lane >> 5)] = t;
  }
  STAMP(1);
  asm volatile("s_waitcnt lgkmcnt(0)" ::: "memory");
  if (lane == 0) __hip_atomic_fetch_add((LAS unsigned*)(lds + L_CT2), 1u, __ATOMIC_RELAXED, __HIP_MEMORY_SCOPE_WORKGROUP);
  volatile LAS unsigned* ctl2 = (volatile LAS unsigned*)(lds + L_CT2);
  const unsigned epoch = __builtin_amdgcn_readfirstlane(ev) + 1u;
  const unsigned tg = epoch & 0xfffu;
  const int rho = (s + g) & 15;
  const bool is_mlp = rho < 9, is_wo = (rho >= 9 && rho < 15);
  const int lm = (s >> 3) * 9 + rho;
  const int mj = xcd * 18 + lm;
  const int witem = xcd * 12 + (s >> 3) * 6 + (rho - 9), wh = witem / 3, wcg = witem - wh * 3;
  const u16* wpa; const u16* wpb; size_t sta, stb;
  if (is_mlp) {
    wpa = w1 + (size_t)(4 * (16 * wave + (lane >> 2))) * FF + 32 * mj + 8 * (lane & 3); sta = FF;
    wpb = w2 + (size_t)(32 * mj + 4 * (lane >> 3)) * D + 64 * wave + 8 * (lane & 7); stb = D;
  } else if (is_wo && wave < 8) {
    const int ch = (lane & 31) < 24 ? (lane & 31) : 23;
    wpa = wo + (size_t)(128 * wh + 16 * wave + 8 * (lane >> 5)) * D + 192 * wcg + 8 * ch; sta = D;
    wpb = wpa + 4 * D; stb = D;
  } else {
    wpa = wo; wpb = wo; sta = 0; stb = 0;
  }
  v4u W[8];
  const int mjc = is_mlp ? mj : 0;
  const bool is_fin = is_mlp && (lm < 2 || (lm == 2 && xcd < 2));
  const int fj = lm < 2 ? xcd * 2 + lm : 16 + xcd;
  v2u b1v = __builtin_nontemporal_load((const GAS v2u*)(b1 + 32 * mjc + 4 * (lane >> 3)));
  unsigned b2v = __builtin_nontemporal_load((const GAS u16*)b2 + (is_fin ? fj : 0) * 32 + (lane & 31));
  v2u xq = __builtin_nontemporal_load((const GAS v2u*)(x + 4 * (16 * wave + r)));

  v4u mlq = {0u, 0u, 0u, 0u};
  if (wave < 8) {
    v4u k2[4], k3[4], R[4];
    WAIT_VM(5);
    W[0] = ldnt(wpa); k2[0] = LDK(2, 0);
    WAIT_VM(2);
    W[1] = ldnt(wpa + sta); k2[1] = LDK(2, 1);
    WAIT_VM(2);
    W[2] = ldnt(wpa + 2 * sta); k2[2] = LDK(2, 2);
    WAIT_VM(2);
    W[3] = ldnt(wpa + 3 * sta); k2[3] = LDK(2, 3);
    WAIT_VM(2);
    W[4] = ldnt(wpb); k3[0] = LDK(3, 0);
    WAIT_VM(2);
    W[5] = ldnt(wpb + stb); k3[1] = LDK(3, 1);
    WAIT_VM(2);
    W[6] = ldnt(wpb + 2 * stb); k3[2] = LDK(3, 2);
    WAIT_VM(2);
    W[7] = ldnt(wpb + 3 * stb); k3[3] = LDK(3, 3);
    WAIT_VM(2);
    R[0] = LDK(4, 0);
    WAIT_VM(2);
    R[1] = LDK(4, 1);
    WAIT_VM(2);
    R[2] = LDK(4, 2);
    STAMP(5);
    _Pragma("unroll 1") for (unsigned it = 0; it < 64u * SPIN_CAP; ++it) { if (__builtin_amdgcn_readfirstlane(ctl2[1])) break; __builtin_amdgcn_s_sleep(1); }
    asm volatile("" ::: "memory");
    STAMP(2);
    const v4u qf = *(const LAS v4u*)(lds + L_Q + ((r >> 2) & 1) * 256 + (4 * gq + (r & 3)) * 16);
    const v4u zero4 = {0u, 0u, 0u, 0u};
    const v4u B0 = (r < 8) ? qf : zero4, B1 = (r < 8) ? zero4 : qf;
    LAS float* Sw = (LAS float*)(lds + L_S + wave * 4096);
    const int sidx = gq * 8 + ((r >> 2) & 1) * 4 + (r & 2) + (r >> 3);
    float mx = -INFINITY;
    const v4f z4f = {0.f, 0.f, 0.f, 0.f};
#define K_POST(i_) do { \
      const float keep = (r & 2) ? ((r & 1) ? Db[3] : Db[2]) : ((r & 1) ? Da[1] : Da[0]); \
      const float send = (r & 2) ? ((r & 1) ? Da[3] : Da[2]) : ((r & 1) ? Db[1] : Db[0]); \
      float y = keep + dpp<XOR2>(send); y += dpp<XOR1>(y); \
      const float sc = rbf(rbf(y) * 0.08838834764831845f); \
      mx = fmaxf(mx, sc); \
      if ((r & 1) == 0) Sw[(i_) * 32 + sidx] = sc; } while (0)
    unsigned tgt = (tstart + (unsigned)TZERO) * 16u + 4u * (unsigned)TAU;
#define TSTAMP() do { } while (0)
#define PACE() do { TSTAMP(); if (TAU) { tgt += (unsigned)TAU; while ((int)((unsigned)__builtin_amdgcn_s_memrealtime() * 16u - tgt) < 0) __builtin_amdgcn_s_sleep(1); } } while (0)
#define STEP4(OP0, OP1, OP2, OP3, LC, LN) do { \
      OP0; SB(); R[3] = LC(3); SB(); OP1; SB(); R[0] = LN(0); SB(); OP2; SB(); R[1] = LN(1); SB(); OP3; SB(); R[2] = LN(2); SB(); } while (0)
#define K_TILE(t_, LC, LN) do { v4f Da, Db; \
      STEP4(Da = mfma(R[0], B0, z4f), Da = mfma(R[1], B1, Da), Db = mfma(R[2], B0, z4f), Db = mfma(R[3], B1, Db), LC, LN); \
      K_POST(t_); PACE(); } while (0)
#define K_PRE(t_, X) do { v4f Da = mfma(X[0], B0, z4f); Da = mfma(X[1], B1, Da); v4f Db = mfma(X[2], B0, z4f); Db = mfma(X[3], B1, Db); K_POST(t_); } while (0)
    {
#define LC_(j) LDK(4, j)
#define LN_(j) LDK(5, j)
    K_TILE(4, LC_, LN_);
#undef LC_
#undef LN_
    }
    K_PRE(0, k0); K_PRE(1, k1); K_PRE(2, k2); K_PRE(3, k3);
#pragma unroll 1
    for (int t = 5; t < 31; ++t) {
#define LC_(j) LDK(t, j)
#define LN_(j) LDK(t + 1, j)
      K_TILE(t, LC_, LN_);
#undef LC_
#undef LN_
    }
    {
#define LC_(j) LDK(31, j)
#define LN_(j) LDV(vbase, j)
    K_TILE(31, LC_, LN_);
#undef LC_
#undef LN_
    }
    STAMP(3);
    asm volatile("" ::: "memory");
    mx = fmaxf(mx, dpp<XOR1>(mx)); mx = fmaxf(mx, dpp<XOR2>(mx)); mx = fmaxf(mx, dpp<XOR8>(mx)); mx = x32max(x16max(mx));
    const float m0 = __builtin_bit_cast(float, __builtin_amdgcn_readlane((int)asu(mx), 0)), m1 = __builtin_bit_cast(float, __builtin_amdgcn_readlane((int)asu(mx), 4));
    {
      const float mh = (lane & 1) ? m1 : m0;
      float l = 0.f;
      v4f sv[4];
#pragma unroll
      for (int k = 0; k < 4; ++k) sv[k] = *(const LAS v4f*)((LAS unsigned char*)Sw + (lane + 64 * k) * 16);
#pragma unroll
      for (int k = 0; k < 4; ++k) {
        v4u pk;
#pragma unroll
        for (int e = 0; e < 4; ++e) { const float p = __builtin_amdgcn_exp2f((sv[k][e] - mh) * LOG2E); l += p; pk[e] = cvtpk(p, p); }
        *(LAS v4u*)((LAS unsigned char*)Sw + (lane + 64 * k) * 16) = pk;
      }
      l += dpp<XOR2>(l); l += dpp<ROR4>(l); l += dpp<XOR8>(l); l = x32sum(x16sum(l));
      const float l0 = __builtin_bit_cast(float, __builtin_amdgcn_readlane((int)asu(l), 0)), l1 = __builtin_bit_cast(float, __builtin_amdgcn_readlane((int)asu(l), 1));
      if (lane == 0) *(LAS v4f*)(lds + L_ML + wave * 16) = v4f{m0, l0, m1, l1};
    }
    asm volatile("" ::: "memory");
    unsigned msk[4];
#pragma unroll
    for (int w = 0; w < 4; ++w) msk[w] = ((r >> 2) == w) ? ((r & 1) ? 0xffff0000u : 0x0000ffffu) : 0u;
    const unsigned pql = (unsigned)(gq * 8 + ((r >> 1) & 1) * 4) * 4;
    const LAS unsigned char* pbase = (const LAS unsigned char*)Sw + pql;
    v4f acc = z4f;
    v4u pq = *(const LAS v4u*)(pbase);
#define AOP(l_) v4u{pq[l_] & msk[0], pq[l_] & msk[1], pq[l_] & msk[2], pq[l_] & msk[3]}
#define LCV_(j) LDV(cur__, j)
#define LNV_(j) LDV(nxt__, j)
#define V_TILE(cur_, nxt_, pn_) do { \
      const v4u A0 = AOP(0), A1 = AOP(1), A2 = AOP(2), A3 = AOP(3); \
      pq = *(const LAS v4u*)(pbase + (pn_) * 128); SB(); \
      const unsigned cur__ = (cur_), nxt__ = (nxt_); (void)cur__; \
      STEP4(acc = mfma(A0, R[0], acc), acc = mfma(A1, R[1], acc), acc = mfma(A2, R[2], acc), acc = mfma(A3, R[3], acc), LCV_, LNV_); } while (0)
#define VOWN(n_) (vbase + (unsigned)(n_) * 32768u)
    V_TILE(VOWN(0), VOWN(1), 1); PACE();
    W_PERM();
#pragma unroll 1
    for (int n = 1; n < 31; ++n) { V_TILE(VOWN(n), VOWN(n + 1), n + 1); PACE(); }
    V_TILE(VOWN(31), 0x7f000000u, 0);
    STAMP(4);
    LAS float* oa = (LAS float*)(lds + L_OACC + wave * 1024);
    *(LAS v2f*)(oa + 8 * r + 2 * gq) = v2f{acc[0], acc[1]};
    *(LAS v2f*)(oa + 128 + 8 * r + 2 * gq) = v2f{acc[2], acc[3]};
  } else {
    _Pragma("unroll 1") for (unsigned it = 0; it < 64u * SPIN_CAP; ++it) { if (__builtin_amdgcn_readfirstlane(ctl2[0]) >= 9u) break; }
    asm volatile("" ::: "memory");
    const unsigned qt = epoch << 16;
    {
      float v = 0.f;
#pragma unroll
      for (int w = 0; w < 9; ++w) v += ((const LAS float*)(lds + L_QP))[w * 16 + r];
      const unsigned gr = qt | (cvtpk(v, v) & 0xffffu);
      const v4u g4 = {dppu<0x00>(gr), dppu<0x55>(gr), dppu<0xAA>(gr), dppu<0xFF>(gr)};
      if ((lane & 51) == 0) __builtin_amdgcn_raw_buffer_store_b128(g4, rW, WS_QG + (unsigned)(16 * cb + lane) * 4, 0, AUXQ);
    }
    STAMP(2);
    const unsigned o0 = WS_QG + (unsigned)g * 1024u + (unsigned)lane * 16u;
    const v4u rs4 = {(unsigned)(size_t)ws, (unsigned)((size_t)ws >> 32), WS_END, 0x00020000u};
    v4u pa, pc, pe;
#define QISSUE(A_) asm volatile("buffer_load_dwordx4 %0, %1, %2, 0 offen sc1" : "=&v"(A_) : "v"(o0), "s"(rs4) : "memory")
#define QSTAGE(A_) { \
      asm volatile("s_waitcnt vmcnt(2)" : "+v"(A_) :: "memory"); \
      *(LAS v2u*)(lds + L_Q + lane * 8) = v2u{__builtin_amdgcn_perm(A_[1], A_[0], 0x05040100u), __builtin_amdgcn_perm(A_[3], A_[2], 0x05040100u)}; \
      if (__all((((A_[0] ^ qt) | (A_[1] ^ qt) | (A_[2] ^ qt) | (A_[3] ^ qt)) >> 16) == 0u)) break; \
      QISSUE(A_); }
    QISSUE(pa);
    __builtin_amdgcn_s_sleep(QPS);
    QISSUE(pc);
    __builtin_amdgcn_s_sleep(QPS);
    QISSUE(pe);
    _Pragma("unroll 1") for (unsigned it = 0; it < SPIN_CAP; ++it) {
      QSTAGE(pa);
      QSTAGE(pc);
      QSTAGE(pe);
    }
    asm volatile("s_waitcnt lgkmcnt(0)" ::: "memory");
    if (lane == 0) ctl2[1] = 1u;
    asm volatile("s_waitcnt vmcnt(0)" : "+v"(pa), "+v"(pc), "+v"(pe) :: "memory");
    STAMP(3);
#pragma unroll
    for (int i = 0; i < 4; ++i) { W[i] = ldnt(wpa + i * sta); W[4 + i] = ldnt(wpb + i * stb); }
    W_PERM();
    v4f ml;
    _Pragma("unroll 1") for (unsigned it = 0; it < 64u * SPIN_CAP; ++it) {
      ml = *(const volatile LAS v4f*)(lds + L_ML + (lane & 7) * 16);
      if (__all(asu(ml[1]) != 0u && asu(ml[3]) != 0u)) break;
      __builtin_amdgcn_s_sleep(16);
    }
    STAMP(4);
    float M0 = ml[0], M1 = ml[2];
    M0 = fmaxf(M0, dpp<XOR1>(M0)); M0 = fmaxf(M0, dpp<XOR2>(M0)); M0 = fmaxf(M0, dpp<XOR7>(M0));
    M1 = fmaxf(M1, dpp<XOR1>(M1)); M1 = fmaxf(M1, dpp<XOR2>(M1)); M1 = fmaxf(M1, dpp<XOR7>(M1));
    const float f0 = __builtin_amdgcn_exp2f((ml[0] - M0) * LOG2E), f1 = __builtin_amdgcn_exp2f((ml[2] - M1) * LOG2E);
    float L0 = ml[1] * f0, L1 = ml[3] * f1;
    L0 += dpp<XOR1>(L0); L0 += dpp<XOR2>(L0); L0 += dpp<XOR7>(L0);
    L1 += dpp<XOR1>(L1); L1 += dpp<XOR2>(L1); L1 += dpp<XOR7>(L1);
    if (lane < 8) *(LAS v2f*)(lds + L_F + lane * 8) = v2f{f0, f1};
    mlq = (lane & 1) ? mark4(v4f{M1, L1, 0.f, 0.f}, tg) : mark4(v4f{M0, L0, 0.f, 0.f}, tg);
    STAMP(5);
  }
  asm volatile("" : "+v"(b1v), "+v"(b2v), "+v"(xq));
  BAR();
  if (wave < 8) {
    const int h = wave >> 2;
    const float f = ((const LAS float*)(lds + L_F))[(lane >> 3) * 2 + h];
    v4f o = f * *(const LAS v4f*)(lds + L_OACC + (lane >> 3) * 1024 + wave * 128 + (lane & 7) * 16);
#pragma unroll
    for (int e = 0; e < 4; ++e) { float t = o[e]; t += dpp<XOR8>(t); o[e] = x32sum(x16sum(t)); }
    if (lane < 8) __builtin_amdgcn_raw_buffer_store_b128(mark4(o, tg), rW, WS_P + (unsigned)((2 * g + h) * 16 + s) * 512 + (wave & 3) * 128 + lane * 16, 0, AUXT);
  } else if (lane < 2) __builtin_amdgcn_raw_buffer_store_b128(mlq, rW, WS_ML + (unsigned)((2 * g + lane) * 16 + s) * 16, 0, AUXT);
  STAMP(6);
  if (is_wo) {
    if (wave < 8) {
      v4u pv, mv;
      const unsigned off = WS_P + (unsigned)(wh * 16 + (lane >> 2)) * 512 + wave * 64 + (lane & 3) * 16;
      const unsigned offm = WS_ML + (unsigned)(wh * 16 + (lane >> 2)) * 16;
      _Pragma("unroll 1") for (unsigned it = 0; it < SPIN_CAP; ++it) {
        pv = poll128(rW, off, 0); mv = poll128(rW, offm, 0);
        if (__all((bad4(pv, tg) | bad4(mv, tg)) == 0u)) break;
      }
      STAMP(14);
      const float ms = asf(mv[0] & 0xfffff000u);
      float Mx = fmaxf(ms, dpp<ROR4>(ms)); Mx = fmaxf(Mx, dpp<XOR8>(Mx)); Mx = x32max(x16max(Mx));
      const float wsf = __builtin_amdgcn_exp2f((ms - Mx) * LOG2E);
      float Ls = wsf * asf(mv[1] & 0xfffff000u);
      Ls += dpp<ROR4>(Ls); Ls += dpp<XOR8>(Ls); Ls = x32sum(x16sum(Ls));
      const v4f o = wsf * dec4(pv);
      float t = x32tr(x16tr(o[0], o[2]), x16tr(o[1], o[3]));
      t += dpp<ROR4>(t); t += dpp<XOR8>(t);
      t *= __builtin_amdgcn_rcpf(Ls);
      const auto sw = __builtin_amdgcn_permlane32_swap(asu(t), asu(t), false, false);
      const unsigned pr = cvtpk(asf(sw[0]), asf(sw[1]));
      const auto s2 = __builtin_amdgcn_permlane16_swap(pr, pr, false, false);
      const unsigned p01 = s2[0], p23 = s2[1];
      const bool hi = lane >= 32;
      const unsigned c0 = dppu<0x00>(p01), c1 = dppu<0x55>(p01), c2 = dppu<0xAA>(p01), c3 = dppu<0xFF>(p01);
      const unsigned d0 = dppu<0x00>(p23), d1 = dppu<0x55>(p23), d2 = dppu<0xAA>(p23), d3 = dppu<0xFF>(p23);
      const unsigned a0 = hi ? c2 : c0, a1 = hi ? d2 : d0, a2 = hi ? c3 : c1, a3 = hi ? d3 : d1;
      float acc8[8];
#pragma unroll
      for (int k = 0; k < 4; ++k) {
        acc8[2 * k] = dot2(W[6][k], a3, dot2(W[4][k], a2, dot2(W[2][k], a1, dot2(W[0][k], a0, 0.f))));
        acc8[2 * k + 1] = dot2(W[7][k], a3, dot2(W[5][k], a2, dot2(W[3][k], a1, dot2(W[1][k], a0, 0.f))));
      }
      const v4f y4 = {x32tr(acc8[0], acc8[4]), x32tr(acc8[1], acc8[5]), x32tr(acc8[2], acc8[6]), x32tr(acc8[3], acc8[7])};
      if ((lane & 31) < 24) *(LAS v4f*)(lds + L_R1 + wave * 768 + (lane & 31) * 32 + (lane >> 5) * 16) = y4;
    }
    BAR();
    if (wave < 6) {
      v4f y = *(const LAS v4f*)(lds + L_R1 + (lane >> 3) * 768 + wave * 128 + (lane & 7) * 16);
#pragma unroll
      for (int e = 0; e < 4; ++e) { float t = y[e]; t += dpp<XOR8>(t); y[e] = x32sum(x16sum(t)); }
      if (lane < 8) __builtin_amdgcn_raw_buffer_store_b128(mark4(y, tg), rW, WS_Y + (unsigned)(wh * 576 + wcg * 192) * 4 + wave * 128 + lane * 16, 0, AUXT);
    }
    STAMP(8);
  } else if (is_mlp) {
    v4f hq;
    {
      const unsigned off = WS_Y + (unsigned)(gq * 8 * 576 + (16 * wave + r) * 4) * 4;
      v4u yv[8];
#pragma unroll
      for (int i = 0; i < 8; ++i) yv[i] = v4u{0u, 0u, 0u, 0u};
      unsigned need = 0xffu;
      _Pragma("unroll 1") for (unsigned it = 0; it < SPIN_CAP && need; ++it) {
#pragma unroll
        for (int i = 0; i < 8; ++i) if (need & (1u << i)) yv[i] = poll128(rW, off, i * 2304);
#pragma unroll
        for (int i = 0; i < 8; ++i) if (need & (1u << i)) { if (__all(bad4(yv[i], tg) == 0u)) need &= ~(1u << i); }
        need = __builtin_amdgcn_readfirstlane(need);
      }
      STAMP(14);
      v4f y = ((dec4(yv[0]) + dec4(yv[1])) + (dec4(yv[2]) + dec4(yv[3]))) + ((dec4(yv[4]) + dec4(yv[5])) + (dec4(yv[6]) + dec4(yv[7])));
#pragma unroll
      for (int e = 0; e < 4; ++e) y[e] = x32sum(x16sum(y[e]));
      hq[0] = rbf(bflo(xq[0]) + rbf(y[0])); hq[1] = rbf(bfhi(xq[0]) + rbf(y[1])); hq[2] = rbf(bflo(xq[1]) + rbf(y[2])); hq[3] = rbf(bfhi(xq[1]) + rbf(y[3]));
    }
    const float mw = row16sum((hq[0] + hq[1]) + (hq[2] + hq[3])) * (1.0f / 64.0f);
    const float e0 = hq[0] - mw, e1 = hq[1] - mw, e2 = hq[2] - mw, e3 = hq[3] - mw;
    const float m2w = row16sum((e0 * e0 + e1 * e1) + (e2 * e2 + e3 * e3));
    if (lane == 0) *(LAS v2f*)(lds + L_R1 + wave * 8) = v2f{mw, m2w};
    if (lane < 16) *(LAS v4f*)(lds + L_H + (16 * wave + lane) * 16) = hq;
    BAR();
    STAMP(11);
    {
      const v2f st = *(const LAS v2f*)(lds + L_R1 + (r < 9 ? r : 0) * 8);
      const float mean2 = row16sum(r < 9 ? st[0] : 0.f) * (1.0f / 9.0f);
      const float dm = st[0] - mean2;
      const float var2 = row16sum(r < 9 ? st[1] + 64.0f * dm * dm : 0.f) * (1.0f / D);
      const float mb2 = rbf(mean2), sd2 = rbf(__builtin_amdgcn_sqrtf(rbf(rbf(var2) + 1e-5f)));
      const float rs = __builtin_amdgcn_rcpf(sd2);
      const unsigned n01 = cvtpk(rbf(hq[0] - mb2) * rs, rbf(hq[1] - mb2) * rs), n23 = cvtpk(rbf(hq[2] - mb2) * rs, rbf(hq[3] - mb2) * rs);
      float a[8];
#pragma unroll
      for (int k = 0; k < 4; ++k) { a[2 * k] = dot2(W[2][k], n23, dot2(W[0][k], n01, 0.f)); a[2 * k + 1] = dot2(W[3][k], n23, dot2(W[1][k], n01, 0.f)); }
#pragma unroll
      for (int k = 0; k < 4; ++k) { const float t1 = a[k] + dpp<XOR8>(a[k]), t2 = a[k + 4] + dpp<XOR8>(a[k + 4]); a[k] = (lane & 8) ? t2 : t1; }
#pragma unroll
      for (int k = 0; k < 2; ++k) { const float t1 = a[k] + dpp<XOR7>(a[k]), t2 = a[k + 2] + dpp<XOR7>(a[k + 2]); a[k] = (lane & 4) ? t2 : t1; }
      { const float t1 = a[0] + dpp<XOR2>(a[0]), t2 = a[1] + dpp<XOR2>(a[1]); a[0] = (lane & 2) ? t2 : t1; }
      a[0] += dpp<XOR1>(a[0]);
      if ((lane & 1) == 0) ((LAS float*)(lds + L_R2))[wave * 32 + (lane >> 1)] = a[0];
    }
    STAMP(12);
    BAR();
    STAMP(13);
    {
      v4f hs = {0.f, 0.f, 0.f, 0.f};
#pragma unroll
      for (int w = 0; w < 9; ++w) hs += *(const LAS v4f*)(lds + L_R2 + w * 128 + (lane >> 3) * 16);
      float t0 = rbf(rbf(hs[0]) + bflo(b1v[0])), t1 = rbf(rbf(hs[1]) + bfhi(b1v[0])), t2 = rbf(rbf(hs[2]) + bflo(b1v[1])), t3 = rbf(rbf(hs[3]) + bfhi(b1v[1]));
      t0 = t0 > 0.f ? t0 : 0.f; t1 = t1 > 0.f ? t1 : 0.f; t2 = t2 > 0.f ? t2 : 0.f; t3 = t3 > 0.f ? t3 : 0.f;
      const unsigned u01 = cvtpk(t0 * t0, t1 * t1), u23 = cvtpk(t2 * t2, t3 * t3);
      float a[8];
#pragma unroll
      for (int k = 0; k < 4; ++k) { a[2 * k] = dot2(W[6][k], u23, dot2(W[4][k], u01, 0.f)); a[2 * k + 1] = dot2(W[7][k], u23, dot2(W[5][k], u01, 0.f)); }
      v4f z;
#pragma unroll
      for (int e = 0; e < 4; ++e) { float t = x32tr(a[e], a[e + 4]); t += dpp<XOR8>(t); z[e] = x16sum(t); }
      if ((lane & 24) == 0) __builtin_amdgcn_raw_buffer_store_b128(mark4(z, tg), rW, WS_Z + (unsigned)mj * 2304 + wave * 256 + (lane & 7) * 32 + (lane >> 5) * 16, 0, AUXT);
    }
    STAMP(8);
    if (is_fin) {
      const unsigned off = WS_Z + (unsigned)(tid >> 3) * 2304 + fj * 128 + (tid & 7) * 16;
      v4u z0, z1;
      _Pragma("unroll 1") for (unsigned it = 0; it < SPIN_CAP; ++it) {
        z0 = poll128(rW, off, 0); z1 = poll128(rW, off, 72 * 2304);
        if (__all((bad4(z0, tg) | bad4(z1, tg)) == 0u)) break;
      }
      STAMP(9);
      const v4f zf = dec4(z0) + dec4(z1);
      float t = x32tr(x16tr(zf[0], zf[2]), x16tr(zf[1], zf[3]));
      t += dpp<XOR8>(t);
      if ((lane & 8) == 0) ((LAS float*)(lds + L_R4))[wave * 32 + (lane & 7) * 4 + ((lane >> 4) & 1) * 2 + (lane >> 5)] = t;
      BAR();
      if (tid < 32) {
        float v = 0.f;
#pragma unroll
        for (int w = 0; w < 9; ++w) v += ((const LAS float*)(lds + L_R4))[w * 32 + tid];
        const float hres = ((const LAS float*)(lds + L_H))[fj * 32 + tid];
        const float o = hres + rbf(rbf(v) + bflo(b2v));
        ((GAS u16*)out)[fj * 32 + tid] = (u16)cvtpk(o, o);
        if (fj == 0 && tid == 0) __builtin_amdgcn_raw_buffer_store_b32(epoch, rW, 0, WS_E, AUX_SC1);
      }
      STAMP(10);
    }
  }
}

extern "C" void kernel_launch(void* const* d_in, const int* in_sizes, int n_in, void* d_out, int out_size, void* d_ws, size_t ws_size, hipStream_t stream) {
  hipLaunchKernelGGL(fused_decode, dim3(256), dim3(NTHR), 0, stream,
                     (const u16*)d_in[0], (const u16*)d_in[1], (const u16*)d_in[2], (const u16*)d_in[3], (const u16*)d_in[4],
                     (const u16*)d_in[6], (const u16*)d_in[7], (const u16*)d_in[8], (const u16*)d_in[9], (u16*)d_out, (unsigned char*)d_ws);
}
```

```cpp
#include <hip/hip_runtime.h>
#include <stdint.h>

typedef unsigned short u16;
typedef unsigned long long u64;
typedef unsigned v4u __attribute__((ext_vector_type(4)));
typedef unsigned v2u __attribute__((ext_vector_type(2)));
typedef float v4f __attribute__((ext_vector_type(4)));
typedef float v2f __attribute__((ext_vector_type(2)));
typedef short v8s __attribute__((ext_vector_type(8)));
typedef __bf16 v2bf __attribute__((ext_vector_type(2)));
#define GAS __attribute__((address_space(1)))
#define LAS __attribute__((address_space(3)))

#define DBG_STAMP 0
#define DRY 0
#define DRYT 5500

constexpr int D = 576, FF = 4608, NQ = 4096;
constexpr int NTHR = 768;
constexpr unsigned WS_E = 0, WS_QG = 4096;
constexpr unsigned WS_ML = WS_QG + 16384;
constexpr unsigned WS_P = WS_ML + 8192;
constexpr unsigned WS_Y = WS_P + 32 * 2048 * 4, WS_Z = WS_Y + 8 * 576 * 4;
constexpr unsigned WS_END = WS_Z + 144 * 576 * 4;
constexpr unsigned WS_DBG = 8u << 20;
#define AUXQ 16
#define AUXT 16
#define RING 3
#define TAU 0
#define TZERO 2400
#define PACE_END 28
#define PRE2 1
#define YLIGHT 3
#define YSLEEP 2
#define GMLT 18
#define PPS 2
#define WT_WO 22
#define WT_MLP 31
#define HWT 30
#define EARLYW 0
#define WTH 10
#define WRING 2
#define QPS 8
constexpr int L_S = 0;
constexpr int L_QP = 32768;
constexpr int L_Q = L_QP + 576;
constexpr int L_ML = L_Q + 512;
constexpr int L_CT = L_ML + 128;
constexpr int L_CT2 = L_CT + 16;
constexpr int L_F = L_CT2 + 16;
constexpr int L_OACC = L_F + 128;
constexpr int L_R1 = L_OACC + 8192;
constexpr int L_R2 = L_R1 + 6144;
constexpr int L_R4 = L_R2 + 1280;
constexpr int L_H = L_R4 + 1280;
constexpr int L_USED = L_H + 2304;
constexpr int LDS_BYTES = 84 * 1024;
static_assert(L_USED <= LDS_BYTES, "lds");
static_assert(L_QP % 16 == 0 && L_Q % 16 == 0 && L_ML % 16 == 0 && L_F % 16 == 0 && L_OACC % 16 == 0 && L_R1 % 16 == 0 && L_R2 % 16 == 0 && L_R4 % 16 == 0 && L_H % 16 == 0, "align");

constexpr int AUX_NT = 2, AUX_SC1 = 16;
constexpr unsigned SPIN_CAP = 400000;
constexpr float LOG2E = 1.4426950408889634f;

__device__ __forceinline__ float asf(unsigned u) { return __builtin_bit_cast(float, u); }
__device__ __forceinline__ unsigned asu(float f) { return __builtin_bit_cast(unsigned, f); }
__device__ __forceinline__ float bflo(unsigned u) { return asf(u << 16); }
__device__ __forceinline__ float bfhi(unsigned u) { return asf(u & 0xffff0000u); }
__device__ __forceinline__ unsigned cvtpk(float lo, float hi) { v2f f = {lo, hi}; return __builtin_bit_cast(unsigned, __builtin_convertvector(f, v2bf)); }
__device__ __forceinline__ float rbf(float f) { return asf(cvtpk(f, f) << 16); }
__device__ __forceinline__ float dot2(unsigned a, unsigned b, float c) { return __builtin_amdgcn_fdot2_f32_bf16(__builtin_bit_cast(v2bf, a), __builtin_bit_cast(v2bf, b), c, false); }
template <int CTRL> __device__ __forceinline__ float dpp(float x) { return __builtin_bit_cast(float, __builtin_amdgcn_mov_dpp(__builtin_bit_cast(int, x), CTRL, 0xf, 0xf, true)); }
template <int CTRL> __device__ __forceinline__ unsigned dppu(unsigned x) { return (unsigned)__builtin_amdgcn_mov_dpp((int)x, CTRL, 0xf, 0xf, true); }
constexpr int XOR1 = 0xB1, XOR2 = 0x4E, XOR3 = 0x1B, XOR7 = 0x141, XOR15 = 0x140, XOR8 = 0x128, ROR4 = 0x124;
__device__ __forceinline__ float x16sum(float x) { auto s = __builtin_amdgcn_permlane16_swap(asu(x), asu(x), false, false); return asf(s[0]) + asf(s[1]); }
__device__ __forceinline__ float x32sum(float x) { auto s = __builtin_amdgcn_permlane32_swap(asu(x), asu(x), false, false); return asf(s[0]) + asf(s[1]); }
__device__ __forceinline__ float x16tr(float a, float b) { auto s = __builtin_amdgcn_permlane16_swap(asu(a), asu(b), false, false); return asf(s[0]) + asf(s[1]); }
__device__ __forceinline__ float x32tr(float a, float b) { auto s = __builtin_amdgcn_permlane32_swap(asu(a), asu(b), false, false); return asf(s[0]) + asf(s[1]); }
__device__ __forceinline__ float x16max(float x) { auto s = __builtin_amdgcn_permlane16_swap(asu(x), asu(x), false, false); return fmaxf(asf(s[0]), asf(s[1])); }
__device__ __forceinline__ float x32max(float x) { auto s = __builtin_amdgcn_permlane32_swap(asu(x), asu(x), false, false); return fmaxf(asf(s[0]), asf(s[1])); }
__device__ __forceinline__ float row16sum(float x) { x += dpp<XOR1>(x); x += dpp<XOR2>(x); x += dpp<XOR7>(x); x += dpp<XOR15>(x); return x; }
__device__ __forceinline__ float row16max(float x) { x = fmaxf(x, dpp<XOR1>(x)); x = fmaxf(x, dpp<XOR2>(x)); x = fmaxf(x, dpp<XOR7>(x)); x = fmaxf(x, dpp<XOR15>(x)); return x; }
__device__ __forceinline__ float wavesum(float x) { return x32sum(x16sum(row16sum(x))); }
__device__ __forceinline__ v4f mfma(v4u a, v4u b, v4f c) { return __builtin_amdgcn_mfma_f32_16x16x32_bf16(__builtin_bit_cast(v8s, a), __builtin_bit_cast(v8s, b), c, 0, 0, 0); }
__device__ __forceinline__ v4u ldnt(const void* p) { return __builtin_nontemporal_load((const GAS v4u*)p); }
__device__ __forceinline__ v4u ldc(const void* p) { return *(const GAS v4u*)p; }
__device__ __forceinline__ float fdiv(float a, float b, float rb) { const float q0 = a * rb; return __builtin_fmaf(__builtin_fmaf(-q0, b, a), rb, q0); }
__device__ __forceinline__ v4u poll128(__amdgpu_buffer_rsrc_t r, unsigned off, unsigned soff) {
  asm volatile("" : "+v"(off) :: "memory");
  return __builtin_amdgcn_raw_buffer_load_b128(r, off, soff, AUXT);
}
__device__ __forceinline__ float tr8(const float (&a)[8], int lane) {
  float c[4];
#pragma unroll
  for (int k = 0; k < 4; ++k) { const float t1 = a[k] + dpp<XOR8>(a[k]), t2 = a[k + 4] + dpp<XOR8>(a[k + 4]); c[k] = (lane & 8) ? t2 : t1; }
  return x32tr(x16tr(c[0], c[2]), x16tr(c[1], c[3]));
}
#define SB() __builtin_amdgcn_sched_barrier(0)
#define W_PAIR() do { _Pragma("unroll") for (int p = 0; p < 4; ++p) { v4u lo, hi; \
    _Pragma("unroll") for (int k = 0; k < 4; ++k) { lo[k] = __builtin_amdgcn_perm(W[2 * p + 1][k], W[2 * p][k], 0x05040100u); hi[k] = __builtin_amdgcn_perm(W[2 * p + 1][k], W[2 * p][k], 0x07060302u); } \
    W[2 * p] = lo; W[2 * p + 1] = hi; asm volatile("" : "+v"(W[2 * p]), "+v"(W[2 * p + 1])); } } while (0)
#define W_PERM() do { if (is_mlp) { const int src_ = (r * 4 + gq) * 4; _Pragma("unroll") for (int p = 0; p < 4; ++p) _Pragma("unroll") for (int k = 0; k < 4; ++k) W[p][k] = (unsigned)__builtin_amdgcn_ds_bpermute(src_, (int)W[p][k]); } \
    W_PAIR(); } while (0)
#define WAIT_VM(n) asm volatile("s_waitcnt vmcnt(" #n ")" ::: "memory")
#define RBAR() do { asm volatile("s_waitcnt lgkmcnt(0)" ::: "memory"); if (real) __builtin_amdgcn_s_barrier(); asm volatile("" ::: "memory"); } while (0)
#define BAR() do { asm volatile("s_waitcnt lgkmcnt(0)" ::: "memory"); __builtin_amdgcn_s_barrier(); asm volatile("" ::: "memory"); } while (0)
__device__ __forceinline__ unsigned enc(float f, unsigned tg) { return ((asu(f) + 0x800u) & 0xfffff000u) | tg; }
__device__ __forceinline__ v4u mark4(v4f v, unsigned tg) { v4u r = {enc(v[0], tg), enc(v[1], tg), enc(v[2], tg), enc(v[3], tg)}; return r; }
__device__ __forceinline__ unsigned bad4(v4u v, unsigned tg) { return ((v[0] ^ tg) | (v[1] ^ tg) | (v[2] ^ tg) | (v[3] ^ tg)) & 0xfffu; }
__device__ __forceinline__ v4f dec4(v4u v) { v4u m = v & 0xfffff000u; return __builtin_bit_cast(v4f, m); }

#define STAMP(i) do { } while (0)

__global__ void __launch_bounds__(NTHR, 3)
fused_decode(const u16* __restrict__ x, const u16* __restrict__ Kc, const u16* __restrict__ Vc, const u16* __restrict__ wq,
             const u16* __restrict__ wo, const u16* __restrict__ w1, const u16* __restrict__ b1, const u16* __restrict__ w2,
             const u16* __restrict__ b2, u16* __restrict__ out, unsigned char* ws) {
  __shared__ __attribute__((aligned(16))) unsigned char lds_raw[LDS_BYTES];
  LAS unsigned char* lds = (LAS unsigned char*)lds_raw;
  const int lane = threadIdx.x & 63;
  const int pw = __builtin_amdgcn_readfirstlane(threadIdx.x >> 6);
  const int tw = pw >= 4 ? pw - 4 : (pw == 3 ? 8 : 0);
  const int tt = tw * 64 + lane;
  const int wave = tw;
  const bool real = pw >= 3;
  const int b = blockIdx.x;
  const int g = b >> 4, s = b & 15;
  const int xcd = b & 7, li = b >> 3;
  const int cb = xcd * 32 + li;
  const int gq = lane >> 4, r = lane & 15;
  const __amdgpu_buffer_rsrc_t rW = __builtin_amdgcn_make_buffer_rsrc((void*)ws, 0, WS_END, 0x00020000);
  volatile LAS unsigned* ctl2 = (volatile LAS unsigned*)(lds + L_CT2);
  STAMP(0);
  const int rho = ((b & 31) + (b >> 5)) & 31;
  const bool is_mlp = rho < 18, is_wo = (rho >= 18 && rho < 27);
  const int mj = xcd * 18 + rho;
  const int wcg = rho - 18;
  const int mjc = is_mlp ? mj : 0;
  const bool is_fin = rho < 2 || (rho == 2 && xcd < 2);
  const int fj = rho < 2 ? xcd * 2 + rho : 16 + xcd;
  v4u W[8];
  v2u b1v, xq; unsigned b2v;
  unsigned epoch = 0;
  unsigned c_mf, c_mo, c_po, c_g, c_l1, c_l2, c_so, c_fo, c_l3, c_l4; float c_x, c_b, c_b2;
#define TAIL_CONSTS() do { \
    c_mf = L_F + ((lane >> 3) * 2 + (tw >> 2)) * 4; c_mo = L_OACC + (lane >> 3) * 1024 + tw * 128 + (lane & 7) * 16; \
    c_po = WS_P + (unsigned)((2 * g + (tw >> 2)) * 16 + s) * 512 + (tw & 3) * 128 + lane * 16; \
    const int ec_ = ((lane >> 4) & 1) * 2 + (lane >> 5); \
    if (is_wo) { \
      c_g = WS_P + (unsigned)((4 * xcd + (tw >> 1)) * 16 + 4 * ((lane >> 1) & 3)) * 512 + (tw & 1) * 256 + (2 * (lane >> 3) + (lane & 1)) * 16; \
      c_l1 = L_R1 + (tw * 64 + 8 * (lane & 7) + ((lane >> 3) & 1) * 4 + ec_) * 4; \
      c_l2 = L_R1 + (lane >> 3) * 256 + tw * 128 + (lane & 7) * 16; \
      c_so = WS_Y + (unsigned)(xcd * 576 + wcg * 64) * 4 + tw * 128 + lane * 16; \
    } else { \
      c_g = WS_Y + (unsigned)(gq * 2 * 576 + (16 * tw + r) * 4) * 4; \
      c_l1 = L_R2 + (tw * 32 + (lane >> 1)) * 4; \
      c_l2 = L_R2 + (((lane >> 2) & 1) * 160 + 4 * (lane >> 3) + (lane & 3)) * 4; \
      c_so = WS_Z + (unsigned)mjc * 2304 + tw * 256 + (lane & 7) * 32 + (lane >> 5) * 16; \
    } \
    c_fo = WS_Z + (unsigned)(tt >> 3) * 2304 + fj * 128 + (tt & 7) * 16; \
    c_l3 = L_R4 + (tw * 32 + (lane & 7) * 4 + ec_) * 4; \
    c_l4 = L_H + ((16 * tw + r) * 4 + ec_) * 4; \
    c_x = asf((((ec_ & 2) ? xq[1] : xq[0]) << ((ec_ & 1) ? 0 : 16)) & 0xffff0000u); \
    c_b = asf((((lane & 2) ? b1v[1] : b1v[0]) << ((lane & 1) ? 0 : 16)) & 0xffff0000u); \
    c_b2 = bflo(b2v); \
    asm volatile("" : "+v"(c_mf), "+v"(c_mo), "+v"(c_po), "+v"(c_g), "+v"(c_l1), "+v"(c_l2), "+v"(c_so), "+v"(c_fo), "+v"(c_l3), "+v"(c_l4), "+v"(c_x), "+v"(c_b), "+v"(c_b2)); } while (0)
#define W_PTRS() const u16* wpa; const u16* wpb; size_t sta, stb; \
  if (is_mlp) { \
    wpa = w1 + (size_t)(4 * (16 * tw + (lane >> 2))) * FF + 32 * mj + 8 * (lane & 3); sta = FF; \
    wpb = w2 + (size_t)(32 * mj + 4 * (lane >> 3)) * D + 64 * tw + 8 * (lane & 7); stb = D; \
  } else if (is_wo && tw < 8) { \
    wpa = wo + (size_t)(512 * xcd + 64 * tw + 8 * (lane >> 3)) * D + 64 * wcg + 8 * (lane & 7); sta = D; \
    wpb = wpa + 4 * D; stb = D; \
  } else { wpa = wo; wpb = wo; sta = 0; stb = 0; }
#define SMALL_LOADS() do { b1v = *(const GAS v2u*)(b1 + 32 * mjc + 4 * (lane >> 3)); \
    b2v = ((const GAS u16*)b2)[(is_fin ? fj : 0) * 32 + (lane & 31)]; xq = *(const GAS v2u*)(x + 4 * (16 * tw + r)); } while (0)

  if (pw < 3) {
    const int qrow = 192 * pw + (lane >> 1);
    const u16* wp = wq + (size_t)qrow * NQ + 16 * cb + 8 * (lane & 1);
    v4u wv[6]; unsigned xr[6];
#pragma unroll
    for (int i = 0; i < 6; ++i) wv[i] = ldc(wp + (size_t)i * 32 * NQ);
    const v4u xa = ldc(x + 8 * lane);
    const unsigned xb = ((const GAS u16*)x)[512 + lane];
#pragma unroll
    for (int i = 0; i < 6; ++i) xr[i] = ((const GAS u16*)x)[qrow + 32 * i];
    if (pw == 0 && lane < 10) *(LAS v4u*)(lds + L_ML + lane * 16) = v4u{0u, 0u, 0u, 0u};
    if (pw == 1 && lane < 16) *(LAS v4u*)(lds + ((lane & 8) ? L_R4 : L_R2) + 1152 + (lane & 7) * 16) = v4u{0u, 0u, 0u, 0u};
    SB();
    BAR();
    SB();
    const unsigned ones = 0x3f803f80u;
    const float xe = bflo(xb);
    const float mean = wavesum(dot2(xa[0], ones, dot2(xa[1], ones, xe)) + dot2(xa[2], ones, dot2(xa[3], ones, 0.f))) * (1.0f / D);
    float d = xe - mean, sq = d * d, sq2 = 0.f;
#pragma unroll
    for (int k = 0; k < 4; ++k) { const float d0 = bflo(xa[k]) - mean, d1 = bfhi(xa[k]) - mean; sq += d0 * d0; sq2 += d1 * d1; }
    const float var = wavesum(sq + sq2) * (1.0f / D);
    const float mb = rbf(mean), sd = rbf(__builtin_sqrtf(rbf(rbf(var) + 1e-5f))), rsd = __builtin_amdgcn_rcpf(sd);
    float a[8];
#pragma unroll
    for (int k = 0; k < 8; ++k) a[k] = 0.f;
#pragma unroll
    for (int p = 0; p < 3; ++p) {
      const unsigned xp = cvtpk(fdiv(rbf(bflo(xr[2 * p]) - mb), sd, rsd), fdiv(rbf(bflo(xr[2 * p + 1]) - mb), sd, rsd));
#pragma unroll
      for (int k = 0; k < 4; ++k) {
        a[2 * k] = dot2(__builtin_amdgcn_perm(wv[2 * p + 1][k], wv[2 * p][k], 0x05040100u), xp, a[2 * k]);
        a[2 * k + 1] = dot2(__builtin_amdgcn_perm(wv[2 * p + 1][k], wv[2 * p][k], 0x07060302u), xp, a[2 * k + 1]);
      }
    }
    float t = tr8(a, lane);
    t += dpp<XOR2>(t);
    t += dpp<XOR3>(dpp<XOR7>(t));
    if ((lane & 6) == 0) ((LAS float*)(lds + L_QP))[pw * 16 + 8 * (lane & 1) + ((lane >> 3) & 1) * 4 + ((lane >> 4) & 1) * 2 + (lane >> 5)] = t;
    asm volatile("s_waitcnt lgkmcnt(0)" ::: "memory");
    if (lane == 0) __hip_atomic_fetch_add((LAS unsigned*)(lds + L_CT2), 1u, __ATOMIC_RELAXED, __HIP_MEMORY_SCOPE_WORKGROUP);
    return;
  } else if (pw >= 4) {
    const __amdgpu_buffer_rsrc_t rK = __builtin_amdgcn_make_buffer_rsrc((void*)(Kc + ((size_t)b << 19)), 0, 1 << 20, 0x00020000);
    const __amdgpu_buffer_rsrc_t rV = __builtin_amdgcn_make_buffer_rsrc((void*)(Vc + ((size_t)b << 19)), 0, 1 << 20, 0x00020000);
    const unsigned wbase = (unsigned)wave * 4096u;
    const unsigned voffK = (unsigned)((r >> 2) * 256 + gq * 64 + (r & 3) * 16), voffV = (unsigned)lane * 16;
#define LDK(tile_, j_) __builtin_amdgcn_raw_buffer_load_b128(rK, voffK + (j_) * 1024, wbase + (unsigned)(tile_) * 32768u, AUX_NT)
#define LDV(off_, j_) __builtin_amdgcn_raw_buffer_load_b128(rV, voffV + (j_) * 1024, (off_), AUX_NT)
    const unsigned vbase = wbase;
    v4u k0[4], k1[4], k2[4], k3[4], R[4];
    unsigned tgt = ((unsigned)__builtin_amdgcn_s_memrealtime() + (unsigned)TZERO) * 16u;
#define PACE() do { if (TAU) { tgt += (unsigned)TAU; while ((int)((unsigned)__builtin_amdgcn_s_memrealtime() * 16u - tgt) < 0) __builtin_amdgcn_s_sleep(1); } } while (0)
    SB();
    BAR();
    SB();
    k0[0] = LDK(0, 0); SB(); k0[1] = LDK(0, 1); SB(); k0[2] = LDK(0, 2);
#define PF(dst_, t_, j_) do { WAIT_VM(2); dst_ = LDK(t_, j_); } while (0)
    PF(k0[3], 0, 3);
    PF(k1[0], 1, 0); PF(k1[1], 1, 1); PF(k1[2], 1, 2); PF(k1[3], 1, 3);
    PF(k2[0], 2, 0); PF(k2[1], 2, 1); PF(k2[2], 2, 2); PF(k2[3], 2, 3);
    PF(k3[0], 3, 0); PF(k3[1], 3, 1); PF(k3[2], 3, 2); PF(k3[3], 3, 3);
    PF(W[0], 4, 0); PF(W[1], 4, 1); PF(W[2], 4, 2); PF(W[3], 4, 3);
    PF(W[4], 5, 0); PF(W[5], 5, 1); PF(W[6], 5, 2); PF(W[7], 5, 3);
    PF(R[0], 6, 0); PF(R[1], 6, 1); PF(R[2], 6, 2);
    STAMP(5);
    _Pragma("unroll 1") for (unsigned it = 0; it < 64u * SPIN_CAP; ++it) { if (__builtin_amdgcn_readfirstlane(ctl2[1])) break; __builtin_amdgcn_s_sleep(1); }
    asm volatile("" ::: "memory");
    epoch = __builtin_amdgcn_readfirstlane(ctl2[2]);
    STAMP(2);
    const v4u qf = *(const LAS v4u*)(lds + L_Q + ((r >> 2) & 1) * 256 + (4 * gq + (r & 3)) * 16);
    const v4u zero4 = {0u, 0u, 0u, 0u};
    const v4u B0 = (r < 8) ? qf : zero4, B1 = (r < 8) ? zero4 : qf;
    LAS float* Sw = (LAS float*)(lds + L_S + wave * 4096);
    const int sidx = gq * 8 + ((r >> 2) & 1) * 4 + (r & 2) + (r >> 3);
    float mx = -INFINITY;
    const v4f z4f = {0.f, 0.f, 0.f, 0.f};
#define K_POST(i_) do { \
      const float keep = (r & 2) ? ((r & 1) ? Db[3] : Db[2]) : ((r & 1) ? Da[1] : Da[0]); \
      const float send = (r & 2) ? ((r & 1) ? Da[3] : Da[2]) : ((r & 1) ? Db[1] : Db[0]); \
      float y = keep + dpp<XOR2>(send); y += dpp<XOR1>(y); \
      const float sc = rbf(rbf(y) * 0.08838834764831845f); \
      mx = fmaxf(mx, sc); \
      if ((r & 1) == 0) Sw[(i_) * 32 + sidx] = sc; } while (0)
#define TSTAMP() do { } while (0)
#define STEP4(OP0, OP1, OP2, OP3, LC, LN) do { \
      OP0; SB(); R[3] = LC(3); SB(); OP1; SB(); R[0] = LN(0); SB(); OP2; SB(); R[1] = LN(1); SB(); OP3; SB(); R[2] = LN(2); SB(); } while (0)
#define K_TILE(t_, LC, LN) do { v4f Da, Db; \
      STEP4(Da = mfma(R[0], B0, z4f), Da = mfma(R[1], B1, Da), Db = mfma(R[2], B0, z4f), Db = mfma(R[3], B1, Db), LC, LN); \
      K_POST(t_); TSTAMP(); } while (0)
#define K_PRE(t_, X) do { v4f Da = mfma(X[0], B0, z4f); Da = mfma(X[1], B1, Da); v4f Db = mfma(X[2], B0, z4f); Db = mfma(X[3], B1, Db); K_POST(t_); } while (0)
#define LC_(j) LDK(t__, j)
#define LN_(j) LDK(t__ + 1, j)
#define KT(t_) do { const int t__ = (t_); K_TILE(t__, LC_, LN_); } while (0)
    KT(6); K_PRE(0, k0);
    KT(7); K_PRE(1, k1);
    KT(8); K_PRE(2, k2);
    KT(9); K_PRE(3, k3);
    KT(10); K_PRE(4, (W + 0));
    KT(11); K_PRE(5, (W + 4));
    KT(12); SMALL_LOADS(); SB();
    KT(13);
#pragma unroll 1
    for (int t = 14; t < 31; ++t) KT(t);
    {
#undef LN_
#define LN_(j) LDV(vbase, j)
      KT(31); tgt += (unsigned)TAU;
    }
    STAMP(3);
    asm volatile("" ::: "memory");
    mx = fmaxf(mx, dpp<XOR1>(mx)); mx = fmaxf(mx, dpp<XOR2>(mx)); mx = fmaxf(mx, dpp<XOR8>(mx)); mx = x32max(x16max(mx));
    const float m0 = __builtin_bit_cast(float, __builtin_amdgcn_readlane((int)asu(mx), 0)), m1 = __builtin_bit_cast(float, __builtin_amdgcn_readlane((int)asu(mx), 4));
    {
      const float mh = (lane & 1) ? m1 : m0;
      float l = 0.f;
      v4f sv[4];
#pragma unroll
      for (int k = 0; k < 4; ++k) sv[k] = *(const LAS v4f*)((LAS unsigned char*)Sw + (lane + 64 * k) * 16);
#pragma unroll
      for (int k = 0; k < 4; ++k) {
        v4u pk;
#pragma unroll
        for (int e = 0; e < 4; ++e) { const float p = __builtin_amdgcn_exp2f((sv[k][e] - mh) * LOG2E); l += p; pk[e] = cvtpk(p, p); }
        *(LAS v4u*)((LAS unsigned char*)Sw + (lane + 64 * k) * 16) = pk;
      }
      l += dpp<XOR2>(l); l += dpp<ROR4>(l); l += dpp<XOR8>(l); l = x32sum(x16sum(l));
      const float l0 = __builtin_bit_cast(float, __builtin_amdgcn_readlane((int)asu(l), 0)), l1 = __builtin_bit_cast(float, __builtin_amdgcn_readlane((int)asu(l), 1));
      if (lane == 0) *(LAS v4f*)(lds + L_ML + wave * 16) = v4f{m0, l0, m1, l1};
    }
    asm volatile("" ::: "memory");
    unsigned msk[4];
#pragma unroll
    for (int w = 0; w < 4; ++w) msk[w] = ((r >> 2) == w) ? ((r & 1) ? 0xffff0000u : 0x0000ffffu) : 0u;
    const unsigned pql = (unsigned)(gq * 8 + ((r >> 1) & 1) * 4) * 4;
    const LAS unsigned char* pbase = (const LAS unsigned char*)Sw + pql;
    v4f acc = z4f;
    v4u pq = *(const LAS v4u*)(pbase);
#define AOP(l_) v4u{pq[l_] & msk[0], pq[l_] & msk[1], pq[l_] & msk[2], pq[l_] & msk[3]}
#define PVM(A_, R_) do { acc = mfma(A_, R_, acc); asm volatile("" : "+v"(acc)); } while (0)
#define LCV_(j) LDV(cur__, j)
#define LNV_(j) LDV(nxt__, j)
#define V_TILE(cur_, nxt_, pn_) do { \
      const v4u A0 = AOP(0), A1 = AOP(1), A2 = AOP(2), A3 = AOP(3); \
      pq = *(const LAS v4u*)(pbase + (pn_) * 128); SB(); \
      const unsigned cur__ = (cur_), nxt__ = (nxt_); (void)cur__; \
      STEP4(PVM(A0, R[0]), PVM(A1, R[1]), PVM(A2, R[2]), PVM(A3, R[3]), LCV_, LNV_); } while (0)
#define VOWN(n_) (vbase + (unsigned)(n_) * 32768u)
    V_TILE(VOWN(0), VOWN(1), 1); TSTAMP();
    TAIL_CONSTS();
    W_PTRS();
#define W_LOADS() do { _Pragma("unroll") for (int i = 0; i < 4; ++i) W[i] = ldc(wpa + i * sta); _Pragma("unroll") for (int i = 0; i < 4; ++i) W[4 + i] = ldnt(wpb + i * stb); SB(); } while (0)
#pragma unroll 1
    for (int n = 1; n < 31; ++n) { V_TILE(VOWN(n), VOWN(n + 1), n + 1); TSTAMP(); if ((n == GMLT || n == HWT) && tt == 0) ctl2[3] = (unsigned)n; }
    V_TILE(VOWN(31), 0x7f000000u, 0);
    if (is_wo) W_LOADS();
    STAMP(4);
    LAS float* oa = (LAS float*)(lds + L_OACC + wave * 1024);
    *(LAS v2f*)(oa + 8 * r + 2 * gq) = v2f{acc[0], acc[1]};
    *(LAS v2f*)(oa + 128 + 8 * r + 2 * gq) = v2f{acc[2], acc[3]};
  } else {
    const unsigned ev = __builtin_amdgcn_raw_buffer_load_b32(rW, 0, WS_E, AUX_SC1);
    SB();
    BAR();
    SB();
    W_PTRS();
    epoch = __builtin_amdgcn_readfirstlane(ev) + 1u;
    const unsigned tg = epoch & 0xfffu;
    _Pragma("unroll 1") for (unsigned it = 0; it < 64u * SPIN_CAP; ++it) { if (__builtin_amdgcn_readfirstlane(ctl2[0]) >= 3u) break; }
    asm volatile("" ::: "memory");
    STAMP(1);
    const unsigned qt = epoch << 16;
    {
      float v = 0.f;
#pragma unroll
      for (int w = 0; w < 3; ++w) v += ((const LAS float*)(lds + L_QP))[w * 16 + r];
      const unsigned gr = qt | (cvtpk(v, v) & 0xffffu);
      const v4u g4 = {dppu<0x00>(gr), dppu<0x55>(gr), dppu<0xAA>(gr), dppu<0xFF>(gr)};
      if ((lane & 51) == 0) __builtin_amdgcn_raw_buffer_store_b128(g4, rW, WS_QG + (unsigned)(16 * cb + lane) * 4, 0, AUXQ);
    }
    STAMP(2);
    const unsigned o0 = WS_QG + (unsigned)g * 1024u + (unsigned)lane * 16u;
    v4u pa, pc, pe;
#define QISSUE(A_) asm volatile("global_load_dwordx4 %0, %1, %2 sc1" : "=&v"(A_) : "v"(o0), "s"(ws) : "memory")
#define QSTAGE(A_) { \
      asm volatile("s_waitcnt vmcnt(2)" : "+v"(A_) :: "memory"); \
      *(LAS v2u*)(lds + L_Q + lane * 8) = v2u{__builtin_amdgcn_perm(A_[1], A_[0], 0x05040100u), __builtin_amdgcn_perm(A_[3], A_[2], 0x05040100u)}; \
      if (__all((((A_[0] ^ qt) | (A_[1] ^ qt) | (A_[2] ^ qt) | (A_[3] ^ qt)) >> 16) == 0u)) break; \
      QISSUE(A_); }
    QISSUE(pa);
    __builtin_amdgcn_s_sleep(QPS);
    QISSUE(pc);
    __builtin_amdgcn_s_sleep(QPS);
    QISSUE(pe);
    _Pragma("unroll 1") for (unsigned it = 0; it < SPIN_CAP; ++it) {
      QSTAGE(pa);
      QSTAGE(pc);
      QSTAGE(pe);
    }
    if (lane == 0) ctl2[2] = epoch;
    asm volatile("s_waitcnt lgkmcnt(0)" ::: "memory");
    if (lane == 0) ctl2[1] = 1u;
    asm volatile("s_waitcnt vmcnt(0)" : "+v"(pa), "+v"(pc), "+v"(pe) :: "memory");
    STAMP(3);
    SMALL_LOADS();
    v4f ml;
    _Pragma("unroll 1") for (unsigned it = 0; it < 64u * SPIN_CAP; ++it) {
      ml = *(const volatile LAS v4f*)(lds + L_ML + (lane & 7) * 16);
      if (__all(asu(ml[1]) != 0u && asu(ml[3]) != 0u)) break;
      __builtin_amdgcn_s_sleep(16);
    }
    STAMP(4);
    float M0 = ml[0], M1 = ml[2];
    M0 = fmaxf(M0, dpp<XOR1>(M0)); M0 = fmaxf(M0, dpp<XOR2>(M0)); M0 = fmaxf(M0, dpp<XOR7>(M0));
    M1 = fmaxf(M1, dpp<XOR1>(M1)); M1 = fmaxf(M1, dpp<XOR2>(M1)); M1 = fmaxf(M1, dpp<XOR7>(M1));
    const float f0 = __builtin_amdgcn_exp2f((ml[0] - M0) * LOG2E), f1 = __builtin_amdgcn_exp2f((ml[2] - M1) * LOG2E);
    float L0 = ml[1] * f0, L1 = ml[3] * f1;
    L0 += dpp<XOR1>(L0); L0 += dpp<XOR2>(L0); L0 += dpp<XOR7>(L0);
    L1 += dpp<XOR1>(L1); L1 += dpp<XOR2>(L1); L1 += dpp<XOR7>(L1);
    _Pragma("unroll 1") for (unsigned it = 0; it < 64u * SPIN_CAP; ++it) { if (__builtin_amdgcn_readfirstlane(ctl2[3])) break; __builtin_amdgcn_s_sleep(16); }
    if (lane == 0) __builtin_amdgcn_raw_buffer_store_b128(v4u{(asu(M0) & 0xfffff000u) | tg, enc(L0, tg), (asu(M1) & 0xfffff000u) | tg, enc(L1, tg)}, rW, WS_ML + (unsigned)b * 16, 0, AUXT);
    v4u gm;
    _Pragma("unroll 1") for (unsigned it = 0; it < SPIN_CAP; ++it) {
      gm = poll128(rW, WS_ML + (unsigned)(16 * g + r) * 16, 0);
      if (__all(bad4(gm, tg) == 0u)) break;
      __builtin_amdgcn_s_sleep(8);
    }
    {
      const v4f gv = dec4(gm);
      const float G0 = row16max(gv[0]), G1 = row16max(gv[2]);
      const float T0 = row16sum(gv[1] * __builtin_amdgcn_exp2f((gv[0] - G0) * LOG2E)), T1 = row16sum(gv[3] * __builtin_amdgcn_exp2f((gv[2] - G1) * LOG2E));
      if (lane < 8) *(LAS v2f*)(lds + L_F + lane * 8) = v2f{__builtin_amdgcn_exp2f((ml[0] - G0) * LOG2E) / T0, __builtin_amdgcn_exp2f((ml[2] - G1) * LOG2E) / T1};
    }
    TAIL_CONSTS();
    STAMP(5);
  }
  const unsigned tg = epoch & 0xfffu;
  BAR();
#define PISSUE(off_, d_) asm volatile("global_load_dwordx4 %0, %2, %3 sc1\n\tglobal_load_dwordx4 %1, %2, %4 sc1" : "+v"(qa_), "+v"(qb_) : "v"(off_), "s"(ws), "s"(ws + (d_)) : "memory")
#define POLL2(RES_, off_, d_) \
      v4u qa_ = {0u, 0u, 0u, 0u}, qb_ = {0u, 0u, 0u, 0u}; \
      PISSUE(off_, d_); __builtin_amdgcn_s_sleep(PPS); \
      _Pragma("unroll 1") for (unsigned it = 0; it < SPIN_CAP; ++it) { \
        PISSUE(off_, d_); \
        asm volatile("s_waitcnt vmcnt(2)" : "+v"(qa_), "+v"(qb_) :: "memory"); \
        if (__all((bad4(qa_, tg) | bad4(qb_, tg)) == 0u)) break; \
      } \
      RES_ = dec4(qa_) + dec4(qb_);
#define PDRAIN() asm volatile("s_waitcnt vmcnt(0)" : "+v"(qa_), "+v"(qb_) :: "memory")
  if (tw < 8) {
    const float f = *(const LAS float*)(lds + c_mf);
    v4f o = f * *(const LAS v4f*)(lds + c_mo);
#pragma unroll
    for (int e = 0; e < 4; ++e) { float t = o[e]; t += dpp<XOR8>(t); o[e] = x32sum(x16sum(t)); }
    if (lane < 8) __builtin_amdgcn_raw_buffer_store_b128(mark4(o, tg), rW, c_po, 0, AUXT);
  }
  STAMP(6);
  if (is_wo) {
    if (tw < 8) {
      W_PAIR();
      const unsigned off = c_g;
      v4u p0, p1, p2, p3;
      _Pragma("unroll 1") for (unsigned it = 0; it < SPIN_CAP; ++it) {
        p0 = poll128(rW, off, 0); p1 = poll128(rW, off, 512); p2 = poll128(rW, off, 1024); p3 = poll128(rW, off, 1536);
        if (__all((bad4(p0, tg) | bad4(p1, tg) | bad4(p2, tg) | bad4(p3, tg)) == 0u)) break;
      }
      STAMP(14);
      const v4f o = (dec4(p0) + dec4(p1)) + (dec4(p2) + dec4(p3));
      const bool b1 = (lane & 2) != 0;
      float u0 = (b1 ? o[2] : o[0]) + dpp<XOR2>(b1 ? o[0] : o[2]), u1 = (b1 ? o[3] : o[1]) + dpp<XOR2>(b1 ? o[1] : o[3]);
      u0 += dpp<XOR3>(dpp<XOR7>(u0)); u1 += dpp<XOR3>(dpp<XOR7>(u1));
      const unsigned pk = cvtpk(u0, u1);
      const unsigned a0 = dppu<0x00>(pk), a1 = dppu<0xAA>(pk), a2 = dppu<0x55>(pk), a3 = dppu<0xFF>(pk);
      float a[8];
#pragma unroll
      for (int k = 0; k < 4; ++k) {
        a[2 * k] = dot2(W[6][k], a3, dot2(W[4][k], a2, dot2(W[2][k], a1, dot2(W[0][k], a0, 0.f))));
        a[2 * k + 1] = dot2(W[7][k], a3, dot2(W[5][k], a2, dot2(W[3][k], a1, dot2(W[1][k], a0, 0.f))));
      }
      const float t = tr8(a, lane);
      *(LAS float*)(lds + c_l1) = t;
    }
    BAR();
    if (tw < 2) {
      v4f y = *(const LAS v4f*)(lds + c_l2);
#pragma unroll
      for (int e = 0; e < 4; ++e) { float t = y[e]; t += dpp<XOR8>(t); y[e] = x32sum(x16sum(t)); }
      if (lane < 8) __builtin_amdgcn_raw_buffer_store_b128(mark4(y, tg), rW, c_so, 0, AUXT);
    }
    STAMP(8);
  } else if (is_mlp) {
    {
      W_PTRS();
      const unsigned offp = WS_P + (unsigned)(2 * g * 16 + r) * 512;
      _Pragma("unroll 1") for (unsigned it = 0; it < SPIN_CAP; ++it) {
        const v4u pv = poll128(rW, offp, 0);
        if (__builtin_popcountll(__ballot(bad4(pv, tg) == 0u)) >= 4 * WTH) break;
        __builtin_amdgcn_s_sleep(2);
      }
#pragma unroll
      for (int i = 0; i < 8; ++i) {
        if (i >= 2) WAIT_VM(1);
        W[i] = i < 4 ? ldc(wpa + i * sta) : ldnt(wpb + (i - 4) * stb);
        asm volatile("" ::: "memory"); SB();
      }
    }
    W_PERM();
    v4f y;
    POLL2(y, c_g, 2304)
    STAMP(14);
    const float hv = rbf(c_x + rbf(x32tr(x16tr(y[0], y[2]), x16tr(y[1], y[3]))));
    const float mw = wavesum(hv) * (1.0f / 64.0f);
    const float e0 = hv - mw;
    const float m2w = wavesum(e0 * e0);
    if (lane == 0) *(LAS v2f*)(lds + L_R1 + tw * 8) = v2f{mw, m2w};
    BAR();
    STAMP(11);
    {
      const v2f st = *(const LAS v2f*)(lds + L_R1 + (r < 9 ? r : 0) * 8);
      if (is_fin) *(LAS float*)(lds + c_l4) = hv;
      const float mean2 = row16sum(r < 9 ? st[0] : 0.f) * (1.0f / 9.0f);
      const float dm = st[0] - mean2;
      const float var2 = row16sum(r < 9 ? st[1] + 64.0f * dm * dm : 0.f) * (1.0f / D);
      const float mb2 = rbf(mean2), sd2 = rbf(__builtin_amdgcn_sqrtf(rbf(rbf(var2) + 1e-5f)));
      const float hn = rbf(hv - mb2) * __builtin_amdgcn_rcpf(sd2);
      const auto sw = __builtin_amdgcn_permlane32_swap(asu(hn), asu(hn), false, false);
      const unsigned pr = cvtpk(asf(sw[0]), asf(sw[1]));
      const auto s2 = __builtin_amdgcn_permlane16_swap(pr, pr, false, false);
      const unsigned n01 = s2[0], n23 = s2[1];
      float a[8];
#pragma unroll
      for (int k = 0; k < 4; ++k) { a[2 * k] = dot2(W[2][k], n23, dot2(W[0][k], n01, 0.f)); a[2 * k + 1] = dot2(W[3][k], n23, dot2(W[1][k], n01, 0.f)); }
#pragma unroll
      for (int k = 0; k < 4; ++k) { const float t1 = a[k] + dpp<XOR8>(a[k]), t2 = a[k + 4] + dpp<XOR8>(a[k + 4]); a[k] = (lane & 8) ? t2 : t1; }
#pragma unroll
      for (int k = 0; k < 2; ++k) { const float t1 = a[k] + dpp<XOR7>(a[k]), t2 = a[k + 2] + dpp<XOR7>(a[k + 2]); a[k] = (lane & 4) ? t2 : t1; }
      { const float t1 = a[0] + dpp<XOR2>(a[0]), t2 = a[1] + dpp<XOR2>(a[1]); a[0] = (lane & 2) ? t2 : t1; }
      a[0] += dpp<XOR1>(a[0]);
      *(LAS float*)(lds + c_l1) = a[0];
    }
    STAMP(12);
    BAR();
    STAMP(13);
    {
      const LAS float* pp = (const LAS float*)(lds + c_l2);
      float hs = ((pp[0] + pp[32]) + (pp[64] + pp[96])) + pp[128];
      hs += dpp<XOR3>(dpp<XOR7>(hs));
      float t0 = rbf(rbf(hs) + c_b);
      t0 = t0 > 0.f ? t0 : 0.f;
      t0 *= t0;
      const unsigned pk = cvtpk(t0, dpp<XOR1>(t0));
      const unsigned u01 = dppu<0x00>(pk), u23 = dppu<0xAA>(pk);
      float a[8];
#pragma unroll
      for (int k = 0; k < 4; ++k) { a[2 * k] = dot2(W[6][k], u23, dot2(W[4][k], u01, 0.f)); a[2 * k + 1] = dot2(W[7][k], u23, dot2(W[5][k], u01, 0.f)); }
      v4f z;
#pragma unroll
      for (int e = 0; e < 4; ++e) { float t = x32tr(a[e], a[e + 4]); t += dpp<XOR8>(t); z[e] = x16sum(t); }
      PDRAIN();
      if ((lane & 24) == 0) __builtin_amdgcn_raw_buffer_store_b128(mark4(z, tg), rW, c_so, 0, AUXT);
    }
    STAMP(8);
    if (is_fin) {
      const unsigned off = c_fo;
      v4f zf;
      POLL2(zf, off, 72 * 2304)
      STAMP(9);
      float t = x32tr(x16tr(zf[0], zf[2]), x16tr(zf[1], zf[3]));
      t += dpp<XOR8>(t);
      *(LAS float*)(lds + c_l3) = t;
      BAR();
      PDRAIN();
      if (tw == 0) {
        const LAS float* pp = (const LAS float*)(lds + L_R4) + (lane >> 5) * 160 + (lane & 31);
        const float v = x32sum(((pp[0] + pp[32]) + (pp[64] + pp[96])) + pp[128]);
        const float hres = ((const LAS float*)(lds + L_H))[fj * 32 + (lane & 31)];
        const float o = hres + rbf(rbf(v) + c_b2);
        if (lane < 32) ((GAS u16*)out)[fj * 32 + lane] = (u16)cvtpk(o, o);
        if (fj == 0 && lane == 0) __builtin_amdgcn_raw_buffer_store_b32(epoch, rW, 0, WS_E, AUX_SC1);
      }
      STAMP(10);
    }
  }
}

extern "C" void kernel_launch(void* const* d_in, const int* in_sizes, int n_in, void* d_out, int out_size, void* d_ws, size_t ws_size, hipStream_t stream) {
  hipLaunchKernelGGL(fused_decode, dim3(256), dim3(NTHR), 0, stream,
                     (const u16*)d_in[0], (const u16*)d_in[1], (const u16*)d_in[2], (const u16*)d_in[3], (const u16*)d_in[4],
                     (const u16*)d_in[6], (const u16*)d_in[7], (const u16*)d_in[8], (const u16*)d_in[9], (u16*)d_out, (unsigned char*)d_ws);
}
```

```cpp
#include <hip/hip_runtime.h>
#include <stdint.h>

typedef unsigned short u16;
typedef unsigned long long u64;
typedef unsigned v4u __attribute__((ext_vector_type(4)));
typedef unsigned v2u __attribute__((ext_vector_type(2)));
typedef float v4f __attribute__((ext_vector_type(4)));
typedef float v2f __attribute__((ext_vector_type(2)));
typedef short v8s __attribute__((ext_vector_type(8)));
typedef __bf16 v2bf __attribute__((ext_vector_type(2)));
#define GAS __attribute__((address_space(1)))
#define LAS __attribute__((address_space(3)))

#define DBG_STAMP 0

constexpr int D = 576, FF = 4608, NQ = 4096;
constexpr int NTHR = 768;
constexpr unsigned WS_E = 0, WS_QG = 4096;
constexpr unsigned WS_ML = WS_QG + 16384;
constexpr unsigned WS_P = WS_ML + 8192;
constexpr unsigned WS_Y = WS_P + 32 * 2048 * 4, WS_Z = WS_Y + 8 * 576 * 4;
constexpr unsigned WS_END = WS_Z + 144 * 576 * 4;
constexpr unsigned WS_DBG = 8u << 20;
#define GMLT 19
#define HWT 23
#define QLAZY 5
#ifndef CU_NOM
#define CU_NOM 1880
#endif
#define TAUN 1756
#define VOFFS 10
#define WRING 3
#define EARLYTH 150
#define LCAP 6
#define PPS 2
constexpr int L_S = 0;
constexpr int L_QP = 32768;
constexpr int L_Q = L_QP + 192;
constexpr int L_ML = L_Q + 512;
constexpr int L_CT2 = L_ML + 128;
constexpr int L_F = L_CT2 + 16;
constexpr int L_OACC = L_F + 64;
constexpr int L_R1 = L_OACC + 8192;
constexpr int L_R2 = L_R1 + 2048;
constexpr int L_R4 = L_R2 + 1280;
constexpr int L_H = L_R4 + 1280;
constexpr int L_USED = L_H + 2304;
constexpr int LDS_BYTES = 84 * 1024;
static_assert(L_USED <= LDS_BYTES, "lds");
static_assert(L_QP % 16 == 0 && L_Q % 16 == 0 && L_ML % 16 == 0 && L_F % 16 == 0 && L_OACC % 16 == 0 && L_R1 % 16 == 0 && L_R2 % 16 == 0 && L_R4 % 16 == 0 && L_H % 16 == 0, "align");

constexpr int AUX_NT = 2, AUX_SC1 = 16;
constexpr unsigned SPIN_CAP = 400000;
constexpr float LOG2E = 1.4426950408889634f;

__device__ __forceinline__ float asf(unsigned u) { return __builtin_bit_cast(float, u); }
__device__ __forceinline__ unsigned asu(float f) { return __builtin_bit_cast(unsigned, f); }
__device__ __forceinline__ float bflo(unsigned u) { return asf(u << 16); }
__device__ __forceinline__ float bfhi(unsigned u) { return asf(u & 0xffff0000u); }
__device__ __forceinline__ unsigned cvtpk(float lo, float hi) { v2f f = {lo, hi}; return __builtin_bit_cast(unsigned, __builtin_convertvector(f, v2bf)); }
__device__ __forceinline__ float rbf(float f) { return asf(cvtpk(f, f) << 16); }
__device__ __forceinline__ float dot2(unsigned a, unsigned b, float c) { return __builtin_amdgcn_fdot2_f32_bf16(__builtin_bit_cast(v2bf, a), __builtin_bit_cast(v2bf, b), c, false); }
template <int CTRL> __device__ __forceinline__ float dpp(float x) { return __builtin_bit_cast(float, __builtin_amdgcn_mov_dpp(__builtin_bit_cast(int, x), CTRL, 0xf, 0xf, true)); }
template <int CTRL> __device__ __forceinline__ unsigned dppu(unsigned x) { return (unsigned)__builtin_amdgcn_mov_dpp((int)x, CTRL, 0xf, 0xf, true); }
constexpr int XOR1 = 0xB1, XOR2 = 0x4E, XOR3 = 0x1B, XOR7 = 0x141, XOR15 = 0x140, XOR8 = 0x128, ROR4 = 0x124;
__device__ __forceinline__ float x16sum(float x) { auto s = __builtin_amdgcn_permlane16_swap(asu(x), asu(x), false, false); return asf(s[0]) + asf(s[1]); }
__device__ __forceinline__ float x32sum(float x) { auto s = __builtin_amdgcn_permlane32_swap(asu(x), asu(x), false, false); return asf(s[0]) + asf(s[1]); }
__device__ __forceinline__ float x16tr(float a, float b) { auto s = __builtin_amdgcn_permlane16_swap(asu(a), asu(b), false, false); return asf(s[0]) + asf(s[1]); }
__device__ __forceinline__ float x32tr(float a, float b) { auto s = __builtin_amdgcn_permlane32_swap(asu(a), asu(b), false, false); return asf(s[0]) + asf(s[1]); }
__device__ __forceinline__ float x16max(float x) { auto s = __builtin_amdgcn_permlane16_swap(asu(x), asu(x), false, false); return fmaxf(asf(s[0]), asf(s[1])); }
__device__ __forceinline__ float x32max(float x) { auto s = __builtin_amdgcn_permlane32_swap(asu(x), asu(x), false, false); return fmaxf(asf(s[0]), asf(s[1])); }
__device__ __forceinline__ float row16sum(float x) { x += dpp<XOR1>(x); x += dpp<XOR2>(x); x += dpp<XOR7>(x); x += dpp<XOR15>(x); return x; }
__device__ __forceinline__ float row16max(float x) { x = fmaxf(x, dpp<XOR1>(x)); x = fmaxf(x, dpp<XOR2>(x)); x = fmaxf(x, dpp<XOR7>(x)); x = fmaxf(x, dpp<XOR15>(x)); return x; }
__device__ __forceinline__ float wavesum(float x) { return x32sum(x16sum(row16sum(x))); }
__device__ __forceinline__ v4f mfma(v4u a, v4u b, v4f c) { return __builtin_amdgcn_mfma_f32_16x16x32_bf16(__builtin_bit_cast(v8s, a), __builtin_bit_cast(v8s, b), c, 0, 0, 0); }
__device__ __forceinline__ v4u ldnt(const void* p) { return __builtin_nontemporal_load((const GAS v4u*)p); }
__device__ __forceinline__ v4u ldc(const void* p) { return *(const GAS v4u*)p; }
__device__ __forceinline__ float fdiv(float a, float b, float rb) { const float q0 = a * rb; return __builtin_fmaf(__builtin_fmaf(-q0, b, a), rb, q0); }
__device__ __forceinline__ v4u poll128(__amdgpu_buffer_rsrc_t r, unsigned off, unsigned soff) {
  asm volatile("" : "+v"(off) :: "memory");
  return __builtin_amdgcn_raw_buffer_load_b128(r, off, soff, AUX_SC1);
}
__device__ __forceinline__ float tr8(const float (&a)[8], int lane) {
  float c[4];
#pragma unroll
  for (int k = 0; k < 4; ++k) { const float t1 = a[k] + dpp<XOR8>(a[k]), t2 = a[k + 4] + dpp<XOR8>(a[k + 4]); c[k] = (lane & 8) ? t2 : t1; }
  return x32tr(x16tr(c[0], c[2]), x16tr(c[1], c[3]));
}
#define SB() __builtin_amdgcn_sched_barrier(0)
#define W_PAIR() do { _Pragma("unroll") for (int p = 0; p < 4; ++p) { v4u lo, hi; \
    _Pragma("unroll") for (int k = 0; k < 4; ++k) { lo[k] = __builtin_amdgcn_perm(W[2 * p + 1][k], W[2 * p][k], 0x05040100u); hi[k] = __builtin_amdgcn_perm(W[2 * p + 1][k], W[2 * p][k], 0x07060302u); } \
    W[2 * p] = lo; W[2 * p + 1] = hi; asm volatile("" : "+v"(W[2 * p]), "+v"(W[2 * p + 1])); } } while (0)
#define W_PERM() do { if (is_mlp) { const int src_ = (r * 4 + gq) * 4; _Pragma("unroll") for (int p = 0; p < 4; ++p) _Pragma("unroll") for (int k = 0; k < 4; ++k) W[p][k] = (unsigned)__builtin_amdgcn_ds_bpermute(src_, (int)W[p][k]); } \
    W_PAIR(); } while (0)
#define WAIT_VM(n) asm volatile("s_waitcnt vmcnt(" #n ")" ::: "memory")
#define BAR() do { asm volatile("s_waitcnt lgkmcnt(0)" ::: "memory"); __builtin_amdgcn_s_barrier(); asm volatile("" ::: "memory"); } while (0)
__device__ __forceinline__ unsigned enc(float f, unsigned tg) { return ((asu(f) + 0x800u) & 0xfffff000u) | tg; }
__device__ __forceinline__ v4u mark4(v4f v, unsigned tg) { v4u r = {enc(v[0], tg), enc(v[1], tg), enc(v[2], tg), enc(v[3], tg)}; return r; }
__device__ __forceinline__ unsigned bad4(v4u v, unsigned tg) { return ((v[0] ^ tg) | (v[1] ^ tg) | (v[2] ^ tg) | (v[3] ^ tg)) & 0xfffu; }
__device__ __forceinline__ v4f dec4(v4u v) { v4u m = v & 0xfffff000u; return __builtin_bit_cast(v4f, m); }

#define STAMP(i) do { } while (0)

__global__ void __launch_bounds__(NTHR, 3)
fused_decode(const u16* __restrict__ x, const u16* __restrict__ Kc, const u16* __restrict__ Vc, const u16* __restrict__ wq,
             const u16* __restrict__ wo, const u16* __restrict__ w1, const u16* __restrict__ b1, const u16* __restrict__ w2,
             const u16* __restrict__ b2, u16* __restrict__ out, unsigned char* ws) {
  __shared__ __attribute__((aligned(16))) unsigned char lds_raw[LDS_BYTES];
  LAS unsigned char* lds = (LAS unsigned char*)lds_raw;
  const int lane = threadIdx.x & 63;
  const int pw = __builtin_amdgcn_readfirstlane(threadIdx.x >> 6);
  const int tw = pw >= 4 ? pw - 4 : (pw == 3 ? 8 : 0);
  const int tt = tw * 64 + lane;
  const int wave = tw;
  const bool real = pw >= 3;
  const int b = blockIdx.x;
  const int g = b >> 4, s = b & 15;
  const int xcd = b & 7, li = b >> 3;
  const int cb = xcd * 32 + li;
  const int gq = lane >> 4, r = lane & 15;
  const __amdgpu_buffer_rsrc_t rW = __builtin_amdgcn_make_buffer_rsrc((void*)ws, 0, WS_END, 0x00020000);
  volatile LAS unsigned* ctl2 = (volatile LAS unsigned*)(lds + L_CT2);
  STAMP(0);
  const int rho = ((b & 31) + (b >> 5)) & 31;
  const bool is_mlp = rho < 18, is_wo = (rho >= 18 && rho < 27);
  const int mj = xcd * 18 + rho;
  const int wcg = rho - 18;
  const int mjc = is_mlp ? mj : 0;
  const bool is_fin = rho < 2 || (rho == 2 && xcd < 2);
  const int fj = rho < 2 ? xcd * 2 + rho : 16 + xcd;
  v4u W[8];
  v2u b1v, xq; unsigned b2v;
  unsigned epoch = 0;
  unsigned c_mf, c_mo, c_po, c_g, c_l1, c_l2, c_so, c_fo, c_l3, c_l4; float c_x, c_b, c_b2;
#define TAIL_CONSTS() do { \
    c_mf = L_F + ((lane >> 3) * 2 + (tw >> 2)) * 4; c_mo = L_OACC + (lane >> 3) * 1024 + tw * 128 + (lane & 7) * 16; \
    c_po = WS_P + (unsigned)((2 * g + (tw >> 2)) * 16 + s) * 512 + (tw & 3) * 128 + lane * 16; \
    const int ec_ = ((lane >> 4) & 1) * 2 + (lane >> 5); \
    if (is_wo) { \
      c_g = WS_P + (unsigned)((4 * xcd + (tw >> 1)) * 16 + 4 * ((lane >> 1) & 3)) * 512 + (tw & 1) * 256 + (2 * (lane >> 3) + (lane & 1)) * 16; \
      c_l1 = L_R1 + (tw * 64 + 8 * (lane & 7) + ((lane >> 3) & 1) * 4 + ec_) * 4; \
      c_l2 = L_R1 + (lane >> 3) * 256 + tw * 128 + (lane & 7) * 16; \
      c_so = WS_Y + (unsigned)(xcd * 576 + wcg * 64) * 4 + tw * 128 + lane * 16; \
    } else { \
      c_g = WS_Y + (unsigned)(gq * 2 * 576 + (16 * tw + r) * 4) * 4; \
      c_l1 = L_R2 + (tw * 32 + (lane >> 1)) * 4; \
      c_l2 = L_R2 + (((lane >> 2) & 1) * 160 + 4 * (lane >> 3) + (lane & 3)) * 4; \
      c_so = WS_Z + (unsigned)mjc * 2304 + tw * 256 + (lane & 7) * 32 + (lane >> 5) * 16; \
    } \
    c_fo = WS_Z + (unsigned)(tt >> 3) * 2304 + fj * 128 + (tt & 7) * 16; \
    c_l3 = L_R4 + (tw * 32 + (lane & 7) * 4 + ec_) * 4; \
    c_l4 = L_H + ((16 * tw + r) * 4 + ec_) * 4; \
    c_x = asf((((ec_ & 2) ? xq[1] : xq[0]) << ((ec_ & 1) ? 0 : 16)) & 0xffff0000u); \
    c_b = asf((((lane & 2) ? b1v[1] : b1v[0]) << ((lane & 1) ? 0 : 16)) & 0xffff0000u); \
    c_b2 = bflo(b2v); \
    asm volatile("" : "+v"(c_mf), "+v"(c_mo), "+v"(c_po), "+v"(c_g), "+v"(c_l1), "+v"(c_l2), "+v"(c_so), "+v"(c_fo), "+v"(c_l3), "+v"(c_l4), "+v"(c_x), "+v"(c_b), "+v"(c_b2)); } while (0)
#define W_PTRS() const u16* wpa; const u16* wpb; size_t sta, stb; \
  if (is_mlp) { \
    wpa = w1 + (size_t)(4 * (16 * tw + (lane >> 2))) * FF + 32 * mj + 8 * (lane & 3); sta = FF; \
    wpb = w2 + (size_t)(32 * mj + 4 * (lane >> 3)) * D + 64 * tw + 8 * (lane & 7); stb = D; \
  } else if (is_wo && tw < 8) { \
    wpa = wo + (size_t)(512 * xcd + 64 * tw + 8 * (lane >> 3)) * D + 64 * wcg + 8 * (lane & 7); sta = D; \
    wpb = wpa + 4 * D; stb = D; \
  } else { wpa = wo; wpb = wo; sta = 0; stb = 0; }
#define SMALL_LOADS() do { b1v = *(const GAS v2u*)(b1 + 32 * mjc + 4 * (lane >> 3)); \
    b2v = ((const GAS u16*)b2)[(is_fin ? fj : 0) * 32 + (lane & 31)]; xq = *(const GAS v2u*)(x + 4 * (16 * tw + r)); } while (0)

  if (pw < 3) {
    const int qrow = 192 * pw + (lane >> 1);
    const u16* wp = wq + (size_t)qrow * NQ + 16 * cb + 8 * (lane & 1);
    v4u wv[6]; unsigned xr[6];
#pragma unroll
    for (int i = 0; i < 6; ++i) wv[i] = ldc(wp + (size_t)i * 32 * NQ);
    const v4u xa = ldc(x + 8 * lane);
    const unsigned xb = ((const GAS u16*)x)[512 + lane];
#pragma unroll
    for (int i = 0; i < 6; ++i) xr[i] = ((const GAS u16*)x)[qrow + 32 * i];
    if (pw == 0 && lane < 9) *(LAS v4u*)(lds + L_ML + lane * 16) = v4u{0u, 0u, 0u, 0u};
    if (pw == 1 && lane < 16) *(LAS v4u*)(lds + ((lane & 8) ? L_R4 : L_R2) + 1152 + (lane & 7) * 16) = v4u{0u, 0u, 0u, 0u};
    SB();
    BAR();
    SB();
    const unsigned ones = 0x3f803f80u;
    const float xe = bflo(xb);
    const float mean = wavesum(dot2(xa[0], ones, dot2(xa[1], ones, xe)) + dot2(xa[2], ones, dot2(xa[3], ones, 0.f))) * (1.0f / D);
    float d = xe - mean, sq = d * d, sq2 = 0.f;
#pragma unroll
    for (int k = 0; k < 4; ++k) { const float d0 = bflo(xa[k]) - mean, d1 = bfhi(xa[k]) - mean; sq += d0 * d0; sq2 += d1 * d1; }
    const float var = wavesum(sq + sq2) * (1.0f / D);
    const float mb = rbf(mean), sd = rbf(__builtin_sqrtf(rbf(rbf(var) + 1e-5f))), rsd = __builtin_amdgcn_rcpf(sd);
    float a[8];
#pragma unroll
    for (int k = 0; k < 8; ++k) a[k] = 0.f;
#pragma unroll
    for (int p = 0; p < 3; ++p) {
      const unsigned xp = cvtpk(fdiv(rbf(bflo(xr[2 * p]) - mb), sd, rsd), fdiv(rbf(bflo(xr[2 * p + 1]) - mb), sd, rsd));
#pragma unroll
      for (int k = 0; k < 4; ++k) {
        a[2 * k] = dot2(__builtin_amdgcn_perm(wv[2 * p + 1][k], wv[2 * p][k], 0x05040100u), xp, a[2 * k]);
        a[2 * k + 1] = dot2(__builtin_amdgcn_perm(wv[2 * p + 1][k], wv[2 * p][k], 0x07060302u), xp, a[2 * k + 1]);
      }
    }
    float t = tr8(a, lane);
    t += dpp<XOR2>(t);
    t += dpp<XOR3>(dpp<XOR7>(t));
    if ((lane & 6) == 0) ((LAS float*)(lds + L_QP))[pw * 16 + 8 * (lane & 1) + ((lane >> 3) & 1) * 4 + ((lane >> 4) & 1) * 2 + (lane >> 5)] = t;
    asm volatile("s_waitcnt lgkmcnt(0)" ::: "memory");
    if (lane == 0) __hip_atomic_fetch_add((LAS unsigned*)(lds + L_CT2), 1u, __ATOMIC_RELAXED, __HIP_MEMORY_SCOPE_WORKGROUP);
    return;
  } else if (pw >= 4) {
    const __amdgpu_buffer_rsrc_t rK = __builtin_amdgcn_make_buffer_rsrc((void*)(Kc + ((size_t)b << 19)), 0, 1 << 20, 0x00020000);
    const __amdgpu_buffer_rsrc_t rV = __builtin_amdgcn_make_buffer_rsrc((void*)(Vc + ((size_t)b << 19)), 0, 1 << 20, 0x00020000);
    const unsigned wbase = (unsigned)wave * 4096u;
    const unsigned voffK = (unsigned)((r >> 2) * 256 + gq * 64 + (r & 3) * 16), voffV = (unsigned)lane * 16;
#define LDK(tile_, j_) __builtin_amdgcn_raw_buffer_load_b128(rK, voffK + (j_) * 1024, wbase + (unsigned)(tile_) * 32768u, AUX_NT)
#define LDV(off_, j_) __builtin_amdgcn_raw_buffer_load_b128(rV, voffV + (j_) * 1024, (off_), AUX_NT)
    const unsigned vbase = wbase;
    v4u k0[4], k1[4], k2[4], k3[4], R[4];
    const unsigned tstart = (unsigned)__builtin_amdgcn_s_memrealtime();
    k0[0] = LDK(0, 0); SB(); k0[1] = LDK(0, 1); SB(); k0[2] = LDK(0, 2);
    SB();
    BAR();
    SB();
#define PF(dst_, t_, j_) do { WAIT_VM(2); dst_ = LDK(t_, j_); } while (0)
    PF(k0[3], 0, 3);
    PF(k1[0], 1, 0); PF(k1[1], 1, 1); PF(k1[2], 1, 2); PF(k1[3], 1, 3);
    PF(k2[0], 2, 0); PF(k2[1], 2, 1); PF(k2[2], 2, 2); PF(k2[3], 2, 3);
    PF(k3[0], 3, 0); PF(k3[1], 3, 1); PF(k3[2], 3, 2); PF(k3[3], 3, 3);
    PF(W[0], 4, 0); PF(W[1], 4, 1); PF(W[2], 4, 2); PF(W[3], 4, 3);
    PF(W[4], 5, 0); PF(W[5], 5, 1); PF(W[6], 5, 2); PF(W[7], 5, 3);
    STAMP(5);
    _Pragma("unroll 1") for (unsigned it = 0; it < 64u * SPIN_CAP; ++it) { if (__builtin_amdgcn_readfirstlane(ctl2[1])) break; __builtin_amdgcn_s_sleep(1); }
    asm volatile("" ::: "memory");
    epoch = __builtin_amdgcn_readfirstlane(ctl2[2]);
    STAMP(2);
    const v4u qf = *(const LAS v4u*)(lds + L_Q + ((r >> 2) & 1) * 256 + (4 * gq + (r & 3)) * 16);
    const v4u zero4 = {0u, 0u, 0u, 0u};
    v4u B0 = (r < 8) ? qf : zero4, B1 = (r < 8) ? zero4 : qf;
    asm volatile("" : "+v"(B0), "+v"(B1));
    PF(R[0], 6, 0); PF(R[1], 6, 1); PF(R[2], 6, 2);
    LAS float* Sw = (LAS float*)(lds + L_S + wave * 4096);
    const int sidx = gq * 8 + ((r >> 2) & 1) * 4 + (r & 2) + (r >> 3);
    float mx = -INFINITY;
    const v4f z4f = {0.f, 0.f, 0.f, 0.f};
#define K_POST(i_) do { \
      const float keep = (r & 2) ? ((r & 1) ? Db[3] : Db[2]) : ((r & 1) ? Da[1] : Da[0]); \
      const float send = (r & 2) ? ((r & 1) ? Da[3] : Da[2]) : ((r & 1) ? Db[1] : Db[0]); \
      float y = keep + dpp<XOR2>(send); y += dpp<XOR1>(y); \
      const float sc = rbf(rbf(y) * 0.08838834764831845f); \
      mx = fmaxf(mx, sc); \
      if ((r & 1) == 0) Sw[(i_) * 32 + sidx] = sc; } while (0)
#define TSTAMP() do { } while (0)
#define STEP4(OP0, OP1, OP2, OP3, LC, LN) do { \
      OP0; SB(); R[3] = LC(3); SB(); OP1; SB(); R[0] = LN(0); SB(); OP2; SB(); R[1] = LN(1); SB(); OP3; SB(); R[2] = LN(2); SB(); } while (0)
#define STEP4B(OP0, OP1, OP2, OP3, LN) do { \
      OP0; SB(); R[0] = LN(0); SB(); OP1; SB(); R[1] = LN(1); SB(); OP2; SB(); R[2] = LN(2); SB(); OP3; SB(); R[3] = LN(3); SB(); } while (0)
#define STEP4C(OP0, OP1, OP2, OP3, LN) do { \
      OP0; SB(); OP1; SB(); R[0] = LN(0); SB(); OP2; SB(); R[1] = LN(1); SB(); OP3; SB(); R[2] = LN(2); SB(); } while (0)
#define K_TILE(t_, LC, LN) do { v4f Da, Db; \
      STEP4(Da = mfma(R[0], B0, z4f), Da = mfma(R[1], B1, Da), Db = mfma(R[2], B0, z4f), Db = mfma(R[3], B1, Db), LC, LN); \
      K_POST(t_); TSTAMP(); } while (0)
#define K_PRE(t_, X) do { v4f Da = mfma(X[0], B0, z4f); Da = mfma(X[1], B1, Da); v4f Db = mfma(X[2], B0, z4f); Db = mfma(X[3], B1, Db); K_POST(t_); } while (0)
#define LC_(j) LDK(t__, j)
#define LN_(j) LDK(t__ + 1, j)
#define KT(t_) do { const int t__ = (t_); K_TILE(t__, LC_, LN_); } while (0)
    KT(6); K_PRE(0, k0);
    KT(7); K_PRE(1, k1);
    KT(8); K_PRE(2, k2);
    KT(9); K_PRE(3, k3);
    KT(10); K_PRE(4, (W + 0));
    KT(11); K_PRE(5, (W + 4));
    KT(12); SMALL_LOADS(); SB();
    KT(13);
    unsigned sched = (tstart + (unsigned)CU_NOM) * 16u;
    int budget = LCAP;
#define LATE() (budget > 0 && (int)((unsigned)__builtin_amdgcn_s_memrealtime() * 16u - sched) > 0)
#define HOLD() do { if (EARLYTH) while ((int)((unsigned)__builtin_amdgcn_s_memrealtime() * 16u - sched) < -(EARLYTH) * 16) __builtin_amdgcn_s_sleep(1); } while (0)
    KT(14);
#pragma unroll 1
    for (int t0 = 15; t0 < 31; t0 += 4) {
      int t = t0;
      HOLD();
      if (LATE()) {
        R[3] = LDK(t0, 3); SB();
#pragma unroll 1
        for (int t__ = t0; t__ < t0 + 3; ++t__) { v4f Da, Db;
          STEP4B(Da = mfma(R[0], B0, z4f), Da = mfma(R[1], B1, Da), Db = mfma(R[2], B0, z4f), Db = mfma(R[3], B1, Db), LN_); K_POST(t__); TSTAMP(); }
        { const int t__ = t0 + 3; v4f Da, Db;
          STEP4C(Da = mfma(R[0], B0, z4f), Da = mfma(R[1], B1, Da), Db = mfma(R[2], B0, z4f), Db = mfma(R[3], B1, Db), LN_); K_POST(t__); TSTAMP(); }
        t = t0 + 4; --budget;
      }
      asm volatile("" : "+s"(t));
#pragma unroll 1
      for (; t < t0 + 4; ++t) KT(t);
      sched += 4u * (unsigned)TAUN;
    }
    {
#undef LN_
#define LN_(j) LDV(vbase, j)
      KT(31);
    }
    STAMP(3);
    asm volatile("" ::: "memory");
    mx = fmaxf(mx, dpp<XOR1>(mx)); mx = fmaxf(mx, dpp<XOR2>(mx)); mx = fmaxf(mx, dpp<XOR8>(mx)); mx = x32max(x16max(mx));
    const float m0 = __builtin_bit_cast(float, __builtin_amdgcn_readlane((int)asu(mx), 0)), m1 = __builtin_bit_cast(float, __builtin_amdgcn_readlane((int)asu(mx), 4));
    {
      const float mh = (lane & 1) ? m1 : m0;
      float l = 0.f;
      v4f sv[4];
#pragma unroll
      for (int k = 0; k < 4; ++k) sv[k] = *(const LAS v4f*)((LAS unsigned char*)Sw + (lane + 64 * k) * 16);
#pragma unroll
      for (int k = 0; k < 4; ++k) {
        v4u pk;
#pragma unroll
        for (int e = 0; e < 4; ++e) { const float p = __builtin_amdgcn_exp2f((sv[k][e] - mh) * LOG2E); l += p; pk[e] = cvtpk(p, p); }
        *(LAS v4u*)((LAS unsigned char*)Sw + (lane + 64 * k) * 16) = pk;
      }
      l += dpp<XOR2>(l); l += dpp<ROR4>(l); l += dpp<XOR8>(l); l = x32sum(x16sum(l));
      const float l0 = __builtin_bit_cast(float, __builtin_amdgcn_readlane((int)asu(l), 0)), l1 = __builtin_bit_cast(float, __builtin_amdgcn_readlane((int)asu(l), 1));
      if (lane == 0) *(LAS v4f*)(lds + L_ML + wave * 16) = v4f{m0, l0, m1, l1};
    }
    asm volatile("" ::: "memory");
    unsigned msk[4];
#pragma unroll
    for (int w = 0; w < 4; ++w) msk[w] = ((r >> 2) == w) ? ((r & 1) ? 0xffff0000u : 0x0000ffffu) : 0u;
    const unsigned pql = (unsigned)(gq * 8 + ((r >> 1) & 1) * 4) * 4;
    const LAS unsigned char* pbase = (const LAS unsigned char*)Sw + pql;
    v4f acc = z4f;
    v4u pq = *(const LAS v4u*)(pbase);
#define AOP(l_) v4u{pq[l_] & msk[0], pq[l_] & msk[1], pq[l_] & msk[2], pq[l_] & msk[3]}
#define PVM(A_, R_) do { acc = mfma(A_, R_, acc); asm volatile("" : "+v"(acc)); } while (0)
#define LCV_(j) LDV(cur__, j)
#define LNV_(j) LDV(nxt__, j)
#define V_TILE(cur_, nxt_, pn_) do { \
      const v4u A0 = AOP(0), A1 = AOP(1), A2 = AOP(2), A3 = AOP(3); \
      pq = *(const LAS v4u*)(pbase + (pn_) * 128); SB(); \
      const unsigned cur__ = (cur_), nxt__ = (nxt_); (void)cur__; \
      STEP4(PVM(A0, R[0]), PVM(A1, R[1]), PVM(A2, R[2]), PVM(A3, R[3]), LCV_, LNV_); } while (0)
#define VOWN(n_) (vbase + (unsigned)(n_) * 32768u)
    V_TILE(VOWN(0), VOWN(1), 1); TSTAMP();
    TAIL_CONSTS();
    W_PTRS();
#define W_LOADS() do { _Pragma("unroll") for (int i = 0; i < 4; ++i) W[i] = ldc(wpa + i * sta); _Pragma("unroll") for (int i = 0; i < 4; ++i) W[4 + i] = ldnt(wpb + i * stb); SB(); } while (0)
#define V_TILEX(STEP, nxt_, pn_) do { \
      const v4u A0 = AOP(0), A1 = AOP(1), A2 = AOP(2), A3 = AOP(3); \
      pq = *(const LAS v4u*)(pbase + (pn_) * 128); SB(); \
      const unsigned nxt__ = (nxt_); \
      STEP(PVM(A0, R[0]), PVM(A1, R[1]), PVM(A2, R[2]), PVM(A3, R[3]), LNV_); } while (0)
    V_TILE(VOWN(1), VOWN(2), 2); TSTAMP();
    V_TILE(VOWN(2), VOWN(3), 3); TSTAMP();
    sched += 4u * (unsigned)TAUN + (unsigned)VOFFS * 16u;
#pragma unroll 1
    for (int n0 = 3; n0 < 31; n0 += 4) {
      if (tt == 0 && n0 >= GMLT) ctl2[3] = (unsigned)n0;
      int m = n0;
      HOLD();
      if (LATE()) {
        R[3] = LDV(VOWN(n0), 3); SB();
#pragma unroll 1
        for (int n = n0; n < n0 + 3; ++n) { V_TILEX(STEP4B, VOWN(n + 1), n + 1); TSTAMP(); }
        V_TILEX(STEP4C, VOWN(n0 + 4), n0 + 4); TSTAMP();
        m = n0 + 4; --budget;
      }
      asm volatile("" : "+s"(m));
#pragma unroll 1
      for (; m < n0 + 4; ++m) { V_TILE(VOWN(m), VOWN(m + 1), m + 1); TSTAMP(); }
      sched += 4u * (unsigned)TAUN;
    }
    V_TILE(VOWN(31), 0x7f000000u, 0);
    if (is_wo) W_LOADS();
    STAMP(4);
    LAS float* oa = (LAS float*)(lds + L_OACC + wave * 1024);
    *(LAS v2f*)(oa + 8 * r + 2 * gq) = v2f{acc[0], acc[1]};
    *(LAS v2f*)(oa + 128 + 8 * r + 2 * gq) = v2f{acc[2], acc[3]};
  } else {
    const unsigned ev = __builtin_amdgcn_raw_buffer_load_b32(rW, 0, WS_E, AUX_SC1);
    SB();
    BAR();
    SB();
    epoch = __builtin_amdgcn_readfirstlane(ev) + 1u;
    const unsigned tg = epoch & 0xfffu;
    _Pragma("unroll 1") for (unsigned it = 0; it < 64u * SPIN_CAP; ++it) { if (__builtin_amdgcn_readfirstlane(ctl2[0]) >= 3u) break; }
    asm volatile("" ::: "memory");
    STAMP(1);
    const unsigned qt = epoch << 16;
    {
      float v = 0.f;
#pragma unroll
      for (int w = 0; w < 3; ++w) v += ((const LAS float*)(lds + L_QP))[w * 16 + r];
      const unsigned gr = qt | (cvtpk(v, v) & 0xffffu);
      const v4u g4 = {dppu<0x00>(gr), dppu<0x55>(gr), dppu<0xAA>(gr), dppu<0xFF>(gr)};
      if ((lane & 51) == 0) __builtin_amdgcn_raw_buffer_store_b128(g4, rW, WS_QG + (unsigned)(16 * cb + lane) * 4, 0, AUX_SC1);
    }
    STAMP(2);
    const unsigned o0 = WS_QG + (unsigned)g * 1024u + (unsigned)lane * 16u;
    v4u pa;
    _Pragma("unroll 1") for (int i = 0; i < QLAZY; ++i) __builtin_amdgcn_s_sleep(16);
    _Pragma("unroll 1") for (unsigned it = 0; it < SPIN_CAP; ++it) {
      pa = poll128(rW, o0, 0);
      if (__all((((pa[0] ^ qt) | (pa[1] ^ qt) | (pa[2] ^ qt) | (pa[3] ^ qt)) >> 16) == 0u)) break;
      __builtin_amdgcn_s_sleep(8);
    }
    *(LAS v2u*)(lds + L_Q + lane * 8) = v2u{__builtin_amdgcn_perm(pa[1], pa[0], 0x05040100u), __builtin_amdgcn_perm(pa[3], pa[2], 0x05040100u)};
    if (lane == 0) ctl2[2] = epoch;
    asm volatile("s_waitcnt lgkmcnt(0)" ::: "memory");
    if (lane == 0) ctl2[1] = 1u;
    STAMP(3);
    SMALL_LOADS();
    v4f ml;
    _Pragma("unroll 1") for (unsigned it = 0; it < 64u * SPIN_CAP; ++it) {
      ml = *(const volatile LAS v4f*)(lds + L_ML + (lane & 7) * 16);
      if (__all(asu(ml[1]) != 0u && asu(ml[3]) != 0u)) break;
      __builtin_amdgcn_s_sleep(16);
    }
    STAMP(4);
    float M0 = ml[0], M1 = ml[2];
    M0 = fmaxf(M0, dpp<XOR1>(M0)); M0 = fmaxf(M0, dpp<XOR2>(M0)); M0 = fmaxf(M0, dpp<XOR7>(M0));
    M1 = fmaxf(M1, dpp<XOR1>(M1)); M1 = fmaxf(M1, dpp<XOR2>(M1)); M1 = fmaxf(M1, dpp<XOR7>(M1));
    const float f0 = __builtin_amdgcn_exp2f((ml[0] - M0) * LOG2E), f1 = __builtin_amdgcn_exp2f((ml[2] - M1) * LOG2E);
    float L0 = ml[1] * f0, L1 = ml[3] * f1;
    L0 += dpp<XOR1>(L0); L0 += dpp<XOR2>(L0); L0 += dpp<XOR7>(L0);
    L1 += dpp<XOR1>(L1); L1 += dpp<XOR2>(L1); L1 += dpp<XOR7>(L1);
    _Pragma("unroll 1") for (unsigned it = 0; it < 64u * SPIN_CAP; ++it) { if (__builtin_amdgcn_readfirstlane(ctl2[3])) break; __builtin_amdgcn_s_sleep(16); }
    if (lane == 0) __builtin_amdgcn_raw_buffer_store_b128(v4u{(asu(M0) & 0xfffff000u) | tg, enc(L0, tg), (asu(M1) & 0xfffff000u) | tg, enc(L1, tg)}, rW, WS_ML + (unsigned)b * 16, 0, AUX_SC1);
    v4u gm;
    _Pragma("unroll 1") for (unsigned it = 0; it < 64u * SPIN_CAP; ++it) { if (__builtin_amdgcn_readfirstlane(ctl2[3]) >= (unsigned)HWT) break; __builtin_amdgcn_s_sleep(8); }
    _Pragma("unroll 1") for (unsigned it = 0; it < SPIN_CAP; ++it) {
      gm = poll128(rW, WS_ML + (unsigned)(16 * g + r) * 16, 0);
      if (__all(bad4(gm, tg) == 0u)) break;
      __builtin_amdgcn_s_sleep(8);
    }
    {
      const v4f gv = dec4(gm);
      const float G0 = row16max(gv[0]), G1 = row16max(gv[2]);
      const float T0 = row16sum(gv[1] * __builtin_amdgcn_exp2f((gv[0] - G0) * LOG2E)), T1 = row16sum(gv[3] * __builtin_amdgcn_exp2f((gv[2] - G1) * LOG2E));
      if (lane < 8) *(LAS v2f*)(lds + L_F + lane * 8) = v2f{__builtin_amdgcn_exp2f((ml[0] - G0) * LOG2E) / T0, __builtin_amdgcn_exp2f((ml[2] - G1) * LOG2E) / T1};
    }
    TAIL_CONSTS();
    STAMP(5);
  }
  const unsigned tg = epoch & 0xfffu;
  BAR();
#define PISSUE(off_, d_) asm volatile("global_load_dwordx4 %0, %2, %3 sc1\n\tglobal_load_dwordx4 %1, %2, %4 sc1" : "+v"(qa_), "+v"(qb_) : "v"(off_), "s"(ws), "s"(ws + (d_)) : "memory")
#define POLL2(RES_, off_, d_) \
      v4u qa_ = {0u, 0u, 0u, 0u}, qb_ = {0u, 0u, 0u, 0u}; \
      PISSUE(off_, d_); __builtin_amdgcn_s_sleep(PPS); \
      _Pragma("unroll 1") for (unsigned it = 0; it < SPIN_CAP; ++it) { \
        PISSUE(off_, d_); \
        asm volatile("s_waitcnt vmcnt(2)" : "+v"(qa_), "+v"(qb_) :: "memory"); \
        if (__all((bad4(qa_, tg) | bad4(qb_, tg)) == 0u)) break; \
      } \
      RES_ = dec4(qa_) + dec4(qb_);
#define PDRAIN() asm volatile("s_waitcnt vmcnt(0)" : "+v"(qa_), "+v"(qb_) :: "memory")
  if (tw < 8) {
    const float f = *(const LAS float*)(lds + c_mf);
    v4f o = f * *(const LAS v4f*)(lds + c_mo);
#pragma unroll
    for (int e = 0; e < 4; ++e) { float t = o[e]; t += dpp<XOR8>(t); o[e] = x32sum(x16sum(t)); }
    if (lane < 8) __builtin_amdgcn_raw_buffer_store_b128(mark4(o, tg), rW, c_po, 0, AUX_SC1);
  }
  STAMP(6);
  if (is_wo) {
    if (tw < 8) {
      W_PAIR();
      const unsigned off = c_g;
      v4u p0, p1, p2, p3;
      _Pragma("unroll 1") for (unsigned it = 0; it < SPIN_CAP; ++it) {
        p0 = poll128(rW, off, 0); p1 = poll128(rW, off, 512); p2 = poll128(rW, off, 1024); p3 = poll128(rW, off, 1536);
        if (__all((bad4(p0, tg) | bad4(p1, tg) | bad4(p2, tg) | bad4(p3, tg)) == 0u)) break;
      }
      STAMP(14);
      const v4f o = (dec4(p0) + dec4(p1)) + (dec4(p2) + dec4(p3));
      const bool b1 = (lane & 2) != 0;
      float u0 = (b1 ? o[2] : o[0]) + dpp<XOR2>(b1 ? o[0] : o[2]), u1 = (b1 ? o[3] : o[1]) + dpp<XOR2>(b1 ? o[1] : o[3]);
      u0 += dpp<XOR3>(dpp<XOR7>(u0)); u1 += dpp<XOR3>(dpp<XOR7>(u1));
      const unsigned pk = cvtpk(u0, u1);
      const unsigned a0 = dppu<0x00>(pk), a1 = dppu<0xAA>(pk), a2 = dppu<0x55>(pk), a3 = dppu<0xFF>(pk);
      float a[8];
#pragma unroll
      for (int k = 0; k < 4; ++k) {
        a[2 * k] = dot2(W[6][k], a3, dot2(W[4][k], a2, dot2(W[2][k], a1, dot2(W[0][k], a0, 0.f))));
        a[2 * k + 1] = dot2(W[7][k], a3, dot2(W[5][k], a2, dot2(W[3][k], a1, dot2(W[1][k], a0, 0.f))));
      }
      const float t = tr8(a, lane);
      *(LAS float*)(lds + c_l1) = t;
    }
    BAR();
    if (tw < 2) {
      v4f y = *(const LAS v4f*)(lds + c_l2);
#pragma unroll
      for (int e = 0; e < 4; ++e) { float t = y[e]; t += dpp<XOR8>(t); y[e] = x32sum(x16sum(t)); }
      if (lane < 8) __builtin_amdgcn_raw_buffer_store_b128(mark4(y, tg), rW, c_so, 0, AUX_SC1);
    }
    STAMP(8);
  } else if (is_mlp) {
    {
      W_PTRS();
#pragma unroll
      for (int i = 0; i < 8; ++i) {
        if (i >= 3) WAIT_VM(2);
        W[i] = i < 4 ? ldc(wpa + i * sta) : ldnt(wpb + (i - 4) * stb);
        asm volatile("" ::: "memory"); SB();
      }
    }
    W_PERM();
    v4f y;
    POLL2(y, c_g, 2304)
    STAMP(14);
    const float hv = rbf(c_x + rbf(x32tr(x16tr(y[0], y[2]), x16tr(y[1], y[3]))));
    const float mw = wavesum(hv) * (1.0f / 64.0f);
    const float e0 = hv - mw;
    const float m2w = wavesum(e0 * e0);
    if (lane == 0) *(LAS v2f*)(lds + L_R1 + tw * 8) = v2f{mw, m2w};
    BAR();
    STAMP(11);
    {
      const v2f st = *(const LAS v2f*)(lds + L_R1 + (r < 9 ? r : 0) * 8);
      if (is_fin) *(LAS float*)(lds + c_l4) = hv;
      const float mean2 = row16sum(r < 9 ? st[0] : 0.f) * (1.0f / 9.0f);
      const float dm = st[0] - mean2;
      const float var2 = row16sum(r < 9 ? st[1] + 64.0f * dm * dm : 0.f) * (1.0f / D);
      const float mb2 = rbf(mean2), sd2 = rbf(__builtin_amdgcn_sqrtf(rbf(rbf(var2) + 1e-5f)));
      const float hn = rbf(hv - mb2) * __builtin_amdgcn_rcpf(sd2);
      const auto sw = __builtin_amdgcn_permlane32_swap(asu(hn), asu(hn), false, false);
      const unsigned pr = cvtpk(asf(sw[0]), asf(sw[1]));
      const auto s2 = __builtin_amdgcn_permlane16_swap(pr, pr, false, false);
      const unsigned n01 = s2[0], n23 = s2[1];
      float a[8];
#pragma unroll
      for (int k = 0; k < 4; ++k) { a[2 * k] = dot2(W[2][k], n23, dot2(W[0][k], n01, 0.f)); a[2 * k + 1] = dot2(W[3][k], n23, dot2(W[1][k], n01, 0.f)); }
#pragma unroll
      for (int k = 0; k < 4; ++k) { const float t1 = a[k] + dpp<XOR8>(a[k]), t2 = a[k + 4] + dpp<XOR8>(a[k + 4]); a[k] = (lane & 8) ? t2 : t1; }
#pragma unroll
      for (int k = 0; k < 2; ++k) { const float t1 = a[k] + dpp<XOR7>(a[k]), t2 = a[k + 2] + dpp<XOR7>(a[k + 2]); a[k] = (lane & 4) ? t2 : t1; }
      { const float t1 = a[0] + dpp<XOR2>(a[0]), t2 = a[1] + dpp<XOR2>(a[1]); a[0] = (lane & 2) ? t2 : t1; }
      a[0] += dpp<XOR1>(a[0]);
      *(LAS float*)(lds + c_l1) = a[0];
    }
    STAMP(12);
    BAR();
    STAMP(13);
    {
      const LAS float* pp = (const LAS float*)(lds + c_l2);
      float hs = ((pp[0] + pp[32]) + (pp[64] + pp[96])) + pp[128];
      hs += dpp<XOR3>(dpp<XOR7>(hs));
      float t0 = rbf(rbf(hs) + c_b);
      t0 = t0 > 0.f ? t0 : 0.f;
      t0 *= t0;
      const unsigned pk = cvtpk(t0, dpp<XOR1>(t0));
      const unsigned u01 = dppu<0x00>(pk), u23 = dppu<0xAA>(pk);
      float a[8];
#pragma unroll
      for (int k = 0; k < 4; ++k) { a[2 * k] = dot2(W[6][k], u23, dot2(W[4][k], u01, 0.f)); a[2 * k + 1] = dot2(W[7][k], u23, dot2(W[5][k], u01, 0.f)); }
      v4f z;
#pragma unroll
      for (int e = 0; e < 4; ++e) { float t = x32tr(a[e], a[e + 4]); t += dpp<XOR8>(t); z[e] = x16sum(t); }
      PDRAIN();
      if ((lane & 24) == 0) __builtin_amdgcn_raw_buffer_store_b128(mark4(z, tg), rW, c_so, 0, AUX_SC1);
    }
    STAMP(8);
    if (is_fin) {
      const unsigned off = c_fo;
      v4f zf;
      POLL2(zf, off, 72 * 2304)
      STAMP(9);
      float t = x32tr(x16tr(zf[0], zf[2]), x16tr(zf[1], zf[3]));
      t += dpp<XOR8>(t);
      *(LAS float*)(lds + c_l3) = t;
      BAR();
      PDRAIN();
      if (tw == 0) {
        const LAS float* pp = (const LAS float*)(lds + L_R4) + (lane >> 5) * 160 + (lane & 31);
        const float v = x32sum(((pp[0] + pp[32]) + (pp[64] + pp[96])) + pp[128]);
        const float hres = ((const LAS float*)(lds + L_H))[fj * 32 + (lane & 31)];
        const float o = hres + rbf(rbf(v) + c_b2);
        if (lane < 32) ((GAS u16*)out)[fj * 32 + lane] = (u16)cvtpk(o, o);
        if (fj == 0 && lane == 0) __builtin_amdgcn_raw_buffer_store_b32(epoch, rW, 0, WS_E, AUX_SC1);
      }
      STAMP(10);
    }
  }
}

extern "C" void kernel_launch(void* const* d_in, const int* in_sizes, int n_in, void* d_out, int out_size, void* d_ws, size_t ws_size, hipStream_t stream) {
  hipLaunchKernelGGL(fused_decode, dim3(256), dim3(NTHR), 0, stream,
                     (const u16*)d_in[0], (const u16*)d_in[1], (const u16*)d_in[2], (const u16*)d_in[3], (const u16*)d_in[4],
                     (const u16*)d_in[6], (const u16*)d_in[7], (const u16*)d_in[8], (const u16*)d_in[9], (u16*)d_out, (unsigned char*)d_ws);
}
```

```cpp
#include <hip/hip_runtime.h>
#include <stdint.h>

typedef unsigned short u16;
typedef unsigned long long u64;
typedef unsigned v4u __attribute__((ext_vector_type(4)));
typedef unsigned v2u __attribute__((ext_vector_type(2)));
typedef float v4f __attribute__((ext_vector_type(4)));
typedef float v2f __attribute__((ext_vector_type(2)));
typedef short v8s __attribute__((ext_vector_type(8)));
typedef __bf16 v2bf __attribute__((ext_vector_type(2)));
#define GAS __attribute__((address_space(1)))
#define LAS __attribute__((address_space(3)))


constexpr int D = 576, FF = 4608, NQ = 4096;
constexpr int NTHR = 768;
constexpr unsigned WS_E = 0, WS_QG = 4096;
constexpr unsigned WS_ML = WS_QG + 16384;
constexpr unsigned WS_P = WS_ML + 8192;
constexpr unsigned WS_Y = WS_P + 32 * 2048 * 4, WS_Z = WS_Y + 8 * 576 * 4;
constexpr unsigned WS_END = WS_Z + 144 * 576 * 4;
#define GMLT 19
#define HWT 23
#define QLAZY 5
#define CU_NOM 1880
#define TAUN 1756
#define VOFFS 10
#define EARLYTH 150
#define LCAP 6
#define PPS 2
#define MRING 5
#define FSL 24
constexpr int L_S = 0;
constexpr int L_QP = 32768;
constexpr int L_Q = L_QP + 192;
constexpr int L_ML = L_Q + 512;
constexpr int L_CT2 = L_ML + 128;
constexpr int L_F = L_CT2 + 16;
constexpr int L_OACC = L_F + 64;
constexpr int L_R1 = L_OACC + 8192;
constexpr int L_R2 = L_R1 + 2048;
constexpr int L_R4 = L_R2 + 1280;
constexpr int L_H = L_R4 + 1280;
constexpr int L_USED = L_H + 2304;
constexpr int LDS_BYTES = 84 * 1024;
static_assert(L_USED <= LDS_BYTES, "lds");
static_assert(L_QP % 16 == 0 && L_Q % 16 == 0 && L_ML % 16 == 0 && L_F % 16 == 0 && L_OACC % 16 == 0 && L_R1 % 16 == 0 && L_R2 % 16 == 0 && L_R4 % 16 == 0 && L_H % 16 == 0, "align");

constexpr int AUX_NT = 2, AUX_SC1 = 16;
constexpr unsigned SPIN_CAP = 400000;
constexpr float LOG2E = 1.4426950408889634f;

__device__ __forceinline__ float asf(unsigned u) { return __builtin_bit_cast(float, u); }
__device__ __forceinline__ unsigned asu(float f) { return __builtin_bit_cast(unsigned, f); }
__device__ __forceinline__ float bflo(unsigned u) { return asf(u << 16); }
__device__ __forceinline__ float bfhi(unsigned u) { return asf(u & 0xffff0000u); }
__device__ __forceinline__ unsigned cvtpk(float lo, float hi) { v2f f = {lo, hi}; return __builtin_bit_cast(unsigned, __builtin_convertvector(f, v2bf)); }
__device__ __forceinline__ float rbf(float f) { return asf(cvtpk(f, f) << 16); }
__device__ __forceinline__ float dot2(unsigned a, unsigned b, float c) { return __builtin_amdgcn_fdot2_f32_bf16(__builtin_bit_cast(v2bf, a), __builtin_bit_cast(v2bf, b), c, false); }
template <int CTRL> __device__ __forceinline__ float dpp(float x) { return __builtin_bit_cast(float, __builtin_amdgcn_mov_dpp(__builtin_bit_cast(int, x), CTRL, 0xf, 0xf, true)); }
template <int CTRL> __device__ __forceinline__ unsigned dppu(unsigned x) { return (unsigned)__builtin_amdgcn_mov_dpp((int)x, CTRL, 0xf, 0xf, true); }
constexpr int XOR1 = 0xB1, XOR2 = 0x4E, XOR3 = 0x1B, XOR7 = 0x141, XOR15 = 0x140, XOR8 = 0x128, ROR4 = 0x124;
__device__ __forceinline__ float x16sum(float x) { auto s = __builtin_amdgcn_permlane16_swap(asu(x), asu(x), false, false); return asf(s[0]) + asf(s[1]); }
__device__ __forceinline__ float x32sum(float x) { auto s = __builtin_amdgcn_permlane32_swap(asu(x), asu(x), false, false); return asf(s[0]) + asf(s[1]); }
__device__ __forceinline__ float x16tr(float a, float b) { auto s = __builtin_amdgcn_permlane16_swap(asu(a), asu(b), false, false); return asf(s[0]) + asf(s[1]); }
__device__ __forceinline__ float x32tr(float a, float b) { auto s = __builtin_amdgcn_permlane32_swap(asu(a), asu(b), false, false); return asf(s[0]) + asf(s[1]); }
__device__ __forceinline__ float x16max(float x) { auto s = __builtin_amdgcn_permlane16_swap(asu(x), asu(x), false, false); return fmaxf(asf(s[0]), asf(s[1])); }
__device__ __forceinline__ float x32max(float x) { auto s = __builtin_amdgcn_permlane32_swap(asu(x), asu(x), false, false); return fmaxf(asf(s[0]), asf(s[1])); }
__device__ __forceinline__ float row16sum(float x) { x += dpp<XOR1>(x); x += dpp<XOR2>(x); x += dpp<XOR7>(x); x += dpp<XOR15>(x); return x; }
__device__ __forceinline__ float row16max(float x) { x = fmaxf(x, dpp<XOR1>(x)); x = fmaxf(x, dpp<XOR2>(x)); x = fmaxf(x, dpp<XOR7>(x)); x = fmaxf(x, dpp<XOR15>(x)); return x; }
__device__ __forceinline__ float wavesum(float x) { return x32sum(x16sum(row16sum(x))); }
__device__ __forceinline__ v4f mfma(v4u a, v4u b, v4f c) { return __builtin_amdgcn_mfma_f32_16x16x32_bf16(__builtin_bit_cast(v8s, a), __builtin_bit_cast(v8s, b), c, 0, 0, 0); }
__device__ __forceinline__ v4u ldnt(const void* p) { return __builtin_nontemporal_load((const GAS v4u*)p); }
__device__ __forceinline__ v4u ldc(const void* p) { return *(const GAS v4u*)p; }
__device__ __forceinline__ float fdiv(float a, float b, float rb) { const float q0 = a * rb; return __builtin_fmaf(__builtin_fmaf(-q0, b, a), rb, q0); }
__device__ __forceinline__ v4u poll128(__amdgpu_buffer_rsrc_t r, unsigned off, unsigned soff) {
  asm volatile("" : "+v"(off) :: "memory");
  return __builtin_amdgcn_raw_buffer_load_b128(r, off, soff, AUX_SC1);
}
__device__ __forceinline__ float tr8(const float (&a)[8], int lane) {
  float c[4];
#pragma unroll
  for (int k = 0; k < 4; ++k) { const float t1 = a[k] + dpp<XOR8>(a[k]), t2 = a[k + 4] + dpp<XOR8>(a[k + 4]); c[k] = (lane & 8) ? t2 : t1; }
  return x32tr(x16tr(c[0], c[2]), x16tr(c[1], c[3]));
}
#define SB() __builtin_amdgcn_sched_barrier(0)
#define W_PAIR() do { _Pragma("unroll") for (int p = 0; p < 4; ++p) { v4u lo, hi; \
    _Pragma("unroll") for (int k = 0; k < 4; ++k) { lo[k] = __builtin_amdgcn_perm(W[2 * p + 1][k], W[2 * p][k], 0x05040100u); hi[k] = __builtin_amdgcn_perm(W[2 * p + 1][k], W[2 * p][k], 0x07060302u); } \
    W[2 * p] = lo; W[2 * p + 1] = hi; asm volatile("" : "+v"(W[2 * p]), "+v"(W[2 * p + 1])); } } while (0)
#define WAIT_VM(n) asm volatile("s_waitcnt vmcnt(" #n ")" ::: "memory")
#define BAR() do { asm volatile("s_waitcnt lgkmcnt(0)" ::: "memory"); __builtin_amdgcn_s_barrier(); asm volatile("" ::: "memory"); } while (0)
__device__ __forceinline__ unsigned enc(float f, unsigned tg) { return ((asu(f) + 0x800u) & 0xfffff000u) | tg; }
__device__ __forceinline__ v4u mark4(v4f v, unsigned tg) { v4u r = {enc(v[0], tg), enc(v[1], tg), enc(v[2], tg), enc(v[3], tg)}; return r; }
__device__ __forceinline__ unsigned bad4(v4u v, unsigned tg) { return ((v[0] ^ tg) | (v[1] ^ tg) | (v[2] ^ tg) | (v[3] ^ tg)) & 0xfffu; }
__device__ __forceinline__ v4f dec4(v4u v) { v4u m = v & 0xfffff000u; return __builtin_bit_cast(v4f, m); }


__global__ void __launch_bounds__(NTHR, 3)
fused_decode(const u16* __restrict__ x, const u16* __restrict__ Kc, const u16* __restrict__ Vc, const u16* __restrict__ wq,
             const u16* __restrict__ wo, const u16* __restrict__ w1, const u16* __restrict__ b1, const u16* __restrict__ w2,
             const u16* __restrict__ b2, u16* __restrict__ out, unsigned char* ws) {
  __shared__ __attribute__((aligned(16))) unsigned char lds_raw[LDS_BYTES];
  LAS unsigned char* lds = (LAS unsigned char*)lds_raw;
  const int lane = threadIdx.x & 63;
  const int pw = __builtin_amdgcn_readfirstlane(threadIdx.x >> 6);
  const int tw = pw >= 4 ? pw - 4 : (pw == 3 ? 8 : 0);
  const int tt = tw * 64 + lane;
  const int wave = tw;
  const int b = blockIdx.x;
  const int g = b >> 4, s = b & 15;
  const int xcd = b & 7, li = b >> 3;
  const int cb = xcd * 32 + li;
  const int gq = lane >> 4, r = lane & 15;
  const __amdgpu_buffer_rsrc_t rW = __builtin_amdgcn_make_buffer_rsrc((void*)ws, 0, WS_END, 0x00020000);
  volatile LAS unsigned* ctl2 = (volatile LAS unsigned*)(lds + L_CT2);
  const int rho = ((b & 31) + (b >> 5)) & 31;
  const bool is_mlp = rho < 18, is_wo = (rho >= 18 && rho < 27);
  const int mj = xcd * 18 + rho;
  const int wcg = rho - 18;
  const bool is_fin = rho == 27 || rho == 28 || (rho == 29 && xcd < 2);
  const int fj = is_fin ? (rho - 27) * 8 + xcd : 0;
  v4u W[8];
  const u16* wpa = wo;
  unsigned epoch = 0;
  unsigned c_mf, c_mo, c_po, c_g, c_l1, c_l2, c_so;
#define TAIL_CONSTS() do { \
    c_mf = L_F + ((lane >> 3) * 2 + (tw >> 2)) * 4; c_mo = L_OACC + (lane >> 3) * 1024 + tw * 128 + (lane & 7) * 16; \
    c_po = WS_P + (unsigned)((2 * g + (tw >> 2)) * 16 + s) * 512 + (tw & 3) * 128 + lane * 16; \
    c_g = WS_P + (unsigned)((4 * xcd + (tw >> 1)) * 16 + 4 * ((lane >> 1) & 3)) * 512 + (tw & 1) * 256 + (2 * (lane >> 3) + (lane & 1)) * 16; \
    c_l1 = L_R1 + (tw * 64 + 8 * (lane & 7) + ((lane >> 3) & 1) * 4 + ((lane >> 4) & 1) * 2 + (lane >> 5)) * 4; \
    c_l2 = L_R1 + (lane >> 3) * 256 + tw * 128 + (lane & 7) * 16; \
    c_so = WS_Y + (unsigned)(xcd * 576 + wcg * 64) * 4 + tw * 128 + lane * 16; \
    asm volatile("" : "+v"(c_mf), "+v"(c_mo), "+v"(c_po), "+v"(c_g), "+v"(c_l1), "+v"(c_l2), "+v"(c_so)); } while (0)

  if (pw < 3) {
    const int qrow = 192 * pw + (lane >> 1);
    const u16* wp = wq + (size_t)qrow * NQ + 16 * cb + 8 * (lane & 1);
    v4u wv[6]; unsigned xr[6];
#pragma unroll
    for (int i = 0; i < 6; ++i) wv[i] = ldc(wp + (size_t)i * 32 * NQ);
    const v4u xa = ldc(x + 8 * lane);
    const unsigned xb = ((const GAS u16*)x)[512 + lane];
#pragma unroll
    for (int i = 0; i < 6; ++i) xr[i] = ((const GAS u16*)x)[qrow + 32 * i];
    if (pw == 0 && lane < 9) *(LAS v4u*)(lds + L_ML + lane * 16) = v4u{0u, 0u, 0u, 0u};
    if (pw == 1 && lane < 16) *(LAS v4u*)(lds + ((lane & 8) ? L_R4 : L_R2) + 1152 + (lane & 7) * 16) = v4u{0u, 0u, 0u, 0u};
    SB();
    BAR();
    SB();
    const unsigned ones = 0x3f803f80u;
    const float xe = bflo(xb);
    const float mean = wavesum(dot2(xa[0], ones, dot2(xa[1], ones, xe)) + dot2(xa[2], ones, dot2(xa[3], ones, 0.f))) * (1.0f / D);
    float d = xe - mean, sq = d * d, sq2 = 0.f;
#pragma unroll
    for (int k = 0; k < 4; ++k) { const float d0 = bflo(xa[k]) - mean, d1 = bfhi(xa[k]) - mean; sq += d0 * d0; sq2 += d1 * d1; }
    const float var = wavesum(sq + sq2) * (1.0f / D);
    const float mb = rbf(mean), sd = rbf(__builtin_sqrtf(rbf(rbf(var) + 1e-5f))), rsd = __builtin_amdgcn_rcpf(sd);
    float a[8];
#pragma unroll
    for (int k = 0; k < 8; ++k) a[k] = 0.f;
#pragma unroll
    for (int p = 0; p < 3; ++p) {
      const unsigned xp = cvtpk(fdiv(rbf(bflo(xr[2 * p]) - mb), sd, rsd), fdiv(rbf(bflo(xr[2 * p + 1]) - mb), sd, rsd));
#pragma unroll
      for (int k = 0; k < 4; ++k) {
        a[2 * k] = dot2(__builtin_amdgcn_perm(wv[2 * p + 1][k], wv[2 * p][k], 0x05040100u), xp, a[2 * k]);
        a[2 * k + 1] = dot2(__builtin_amdgcn_perm(wv[2 * p + 1][k], wv[2 * p][k], 0x07060302u), xp, a[2 * k + 1]);
      }
    }
    float t = tr8(a, lane);
    t += dpp<XOR2>(t);
    t += dpp<XOR3>(dpp<XOR7>(t));
    if ((lane & 6) == 0) ((LAS float*)(lds + L_QP))[pw * 16 + 8 * (lane & 1) + ((lane >> 3) & 1) * 4 + ((lane >> 4) & 1) * 2 + (lane >> 5)] = t;
    asm volatile("s_waitcnt lgkmcnt(0)" ::: "memory");
    if (lane == 0) __hip_atomic_fetch_add((LAS unsigned*)(lds + L_CT2), 1u, __ATOMIC_RELAXED, __HIP_MEMORY_SCOPE_WORKGROUP);
    if (!is_mlp) return;
    const int ec = ((lane >> 4) & 1) * 2 + (lane >> 5);
    float cx[3];
#pragma unroll
    for (int k = 0; k < 3; ++k) cx[k] = bflo(((const GAS u16*)x)[4 * (16 * (3 * pw + k) + r) + ec]);
    float cbias = bflo(((const GAS u16*)b1)[32 * mj + 4 * (lane >> 3) + (lane & 3)]);
    unsigned cg = WS_Y + (unsigned)(gq * 2 * 576 + (48 * pw + r) * 4) * 4;
    unsigned cl1 = L_R2 + (pw * 32 + (lane >> 1)) * 4, cl2 = L_R2 + (4 * (lane >> 3) + (lane & 3)) * 4;
    unsigned cso = WS_Z + (unsigned)mj * 2304 + pw * 768 + (lane & 7) * 32 + (lane >> 5) * 16;
    wpa = w1 + (size_t)(4 * (48 * pw + (lane >> 2))) * FF + 32 * mj + 8 * (lane & 3);
    const u16* wpb = w2 + (size_t)(32 * mj + 4 * (lane >> 3)) * D + 192 * pw + 8 * (lane & 7);
    asm volatile("" : "+v"(cx[0]), "+v"(cx[1]), "+v"(cx[2]), "+v"(cbias), "+v"(cg), "+v"(cl1), "+v"(cl2), "+v"(cso), "+v"(wpa), "+v"(wpb));
    BAR();
    const unsigned tg = __builtin_amdgcn_readfirstlane(ctl2[2]) & 0xfffu;
    v4u A1[3][4], A2[3][4];
#pragma unroll
    for (int i = 0; i < 24; ++i) {
      if (i >= MRING) asm volatile("s_waitcnt vmcnt(%0)" :: "i"(MRING - 1) : "memory");
      if (i < 12) A1[i >> 2][i & 3] = ldc(wpa + (size_t)((i >> 2) * 64 + (i & 3)) * FF);
      else A2[(i - 12) >> 2][i & 3] = ldnt(wpb + (size_t)(i & 3) * D + ((i - 12) >> 2) * 64);
      asm volatile("" ::: "memory"); SB();
    }
#define PAIR4(X) do { _Pragma("unroll") for (int p = 0; p < 2; ++p) { v4u lo, hi; \
      _Pragma("unroll") for (int k = 0; k < 4; ++k) { lo[k] = __builtin_amdgcn_perm(X[2 * p + 1][k], X[2 * p][k], 0x05040100u); hi[k] = __builtin_amdgcn_perm(X[2 * p + 1][k], X[2 * p][k], 0x07060302u); } \
      X[2 * p] = lo; X[2 * p + 1] = hi; asm volatile("" : "+v"(X[2 * p]), "+v"(X[2 * p + 1])); } } while (0)
#pragma unroll
    for (int q = 0; q < 3; ++q) {
      const int src = (r * 4 + gq) * 4;
#pragma unroll
      for (int p = 0; p < 4; ++p)
#pragma unroll
        for (int k = 0; k < 4; ++k) A1[q][p][k] = (unsigned)__builtin_amdgcn_ds_bpermute(src, (int)A1[q][p][k]);
      PAIR4(A1[q]);
    }
#pragma unroll
    for (int q = 0; q < 3; ++q) PAIR4(A2[q]);
    v4u yv[6];
#pragma unroll
    for (int i = 0; i < 6; ++i) yv[i] = v4u{0u, 0u, 0u, 0u};
#define YISSUE() asm volatile("global_load_dwordx4 %0, %6, %7 sc1\n\tglobal_load_dwordx4 %1, %6, %7 offset:2304 sc1\n\tglobal_load_dwordx4 %2, %6, %7 offset:256 sc1\n\t" \
      "global_load_dwordx4 %3, %6, %7 offset:2560 sc1\n\tglobal_load_dwordx4 %4, %6, %7 offset:512 sc1\n\tglobal_load_dwordx4 %5, %6, %7 offset:2816 sc1" \
      : "+v"(yv[0]), "+v"(yv[1]), "+v"(yv[2]), "+v"(yv[3]), "+v"(yv[4]), "+v"(yv[5]) : "v"(cg), "s"(ws) : "memory")
    YISSUE();
    _Pragma("unroll 1") for (unsigned it = 0; it < SPIN_CAP; ++it) {
      YISSUE();
      asm volatile("s_waitcnt vmcnt(6)" : "+v"(yv[0]), "+v"(yv[1]), "+v"(yv[2]), "+v"(yv[3]), "+v"(yv[4]), "+v"(yv[5]) :: "memory");
      if (__all((bad4(yv[0], tg) | bad4(yv[1], tg) | bad4(yv[2], tg) | bad4(yv[3], tg) | bad4(yv[4], tg) | bad4(yv[5], tg)) == 0u)) break;
    }
    float hv[3];
#pragma unroll
    for (int k = 0; k < 3; ++k) { const v4f y = dec4(yv[2 * k]) + dec4(yv[2 * k + 1]); hv[k] = rbf(cx[k] + rbf(x32tr(x16tr(y[0], y[2]), x16tr(y[1], y[3])))); }
    const float mw = wavesum((hv[0] + hv[1]) + hv[2]) * (1.0f / 192.0f);
    const float e0 = hv[0] - mw, e1 = hv[1] - mw, e2 = hv[2] - mw;
    const float m2w = wavesum((e0 * e0 + e1 * e1) + e2 * e2);
    if (lane == 0) *(LAS v2f*)(lds + L_R1 + pw * 8) = v2f{mw, m2w};
    BAR();
    {
      const v4f sa = *(const LAS v4f*)(lds + L_R1); const v2f sb = *(const LAS v2f*)(lds + L_R1 + 16);
      const float mean2 = ((sa[0] + sa[2]) + sb[0]) * (1.0f / 3.0f);
      const float d0 = sa[0] - mean2, d1 = sa[2] - mean2, d2 = sb[0] - mean2;
      const float var2 = (((sa[1] + sa[3]) + sb[1]) + 192.0f * ((d0 * d0 + d1 * d1) + d2 * d2)) * (1.0f / D);
      const float mb2 = rbf(mean2), rs2 = __builtin_amdgcn_rcpf(rbf(__builtin_amdgcn_sqrtf(rbf(rbf(var2) + 1e-5f))));
      float a[8];
#pragma unroll
      for (int k = 0; k < 8; ++k) a[k] = 0.f;
#pragma unroll
      for (int q = 0; q < 3; ++q) {
        const float hn = rbf(hv[q] - mb2) * rs2;
        const auto sw = __builtin_amdgcn_permlane32_swap(asu(hn), asu(hn), false, false);
        const unsigned pr = cvtpk(asf(sw[0]), asf(sw[1]));
        const auto s2 = __builtin_amdgcn_permlane16_swap(pr, pr, false, false);
#pragma unroll
        for (int k = 0; k < 4; ++k) { a[2 * k] = dot2(A1[q][2][k], s2[1], dot2(A1[q][0][k], s2[0], a[2 * k])); a[2 * k + 1] = dot2(A1[q][3][k], s2[1], dot2(A1[q][1][k], s2[0], a[2 * k + 1])); }
      }
#pragma unroll
      for (int k = 0; k < 4; ++k) { const float t1 = a[k] + dpp<XOR8>(a[k]), t2 = a[k + 4] + dpp<XOR8>(a[k + 4]); a[k] = (lane & 8) ? t2 : t1; }
#pragma unroll
      for (int k = 0; k < 2; ++k) { const float t1 = a[k] + dpp<XOR7>(a[k]), t2 = a[k + 2] + dpp<XOR7>(a[k + 2]); a[k] = (lane & 4) ? t2 : t1; }
      { const float t1 = a[0] + dpp<XOR2>(a[0]), t2 = a[1] + dpp<XOR2>(a[1]); a[0] = (lane & 2) ? t2 : t1; }
      a[0] += dpp<XOR1>(a[0]);
      *(LAS float*)(lds + cl1) = a[0];
    }
    BAR();
    {
      const LAS float* pp = (const LAS float*)(lds + cl2);
      float t0 = rbf(rbf((pp[0] + pp[32]) + pp[64]) + cbias);
      t0 = t0 > 0.f ? t0 : 0.f;
      t0 *= t0;
      const unsigned pk = cvtpk(t0, dpp<XOR1>(t0));
      const unsigned u01 = dppu<0x00>(pk), u23 = dppu<0xAA>(pk);
      asm volatile("s_waitcnt vmcnt(0)" : "+v"(yv[0]), "+v"(yv[1]), "+v"(yv[2]), "+v"(yv[3]), "+v"(yv[4]), "+v"(yv[5]) :: "memory");
#pragma unroll
      for (int q = 0; q < 3; ++q) {
        float a[8];
#pragma unroll
        for (int k = 0; k < 4; ++k) { a[2 * k] = dot2(A2[q][2][k], u23, dot2(A2[q][0][k], u01, 0.f)); a[2 * k + 1] = dot2(A2[q][3][k], u23, dot2(A2[q][1][k], u01, 0.f)); }
        v4f z;
#pragma unroll
        for (int e = 0; e < 4; ++e) { float t = x32tr(a[e], a[e + 4]); t += dpp<XOR8>(t); z[e] = x16sum(t); }
        if ((lane & 24) == 0) __builtin_amdgcn_raw_buffer_store_b128(mark4(z, tg), rW, cso + q * 256, 0, AUX_SC1);
      }
    }
    return;
  } else if (pw >= 4) {
    const __amdgpu_buffer_rsrc_t rK = __builtin_amdgcn_make_buffer_rsrc((void*)(Kc + ((size_t)b << 19)), 0, 1 << 20, 0x00020000);
    const __amdgpu_buffer_rsrc_t rV = __builtin_amdgcn_make_buffer_rsrc((void*)(Vc + ((size_t)b << 19)), 0, 1 << 20, 0x00020000);
    const unsigned wbase = (unsigned)wave * 4096u;
    const unsigned voffK = (unsigned)((r >> 2) * 256 + gq * 64 + (r & 3) * 16), voffV = (unsigned)lane * 16;
#define LDK(tile_, j_) __builtin_amdgcn_raw_buffer_load_b128(rK, voffK + (j_) * 1024, wbase + (unsigned)(tile_) * 32768u, AUX_NT)
#define LDV(off_, j_) __builtin_amdgcn_raw_buffer_load_b128(rV, voffV + (j_) * 1024, (off_), AUX_NT)
    const unsigned vbase = wbase;
    v4u k0[4], k1[4], k2[4], k3[4], R[4];
    const unsigned tstart = (unsigned)__builtin_amdgcn_s_memrealtime();
    k0[0] = LDK(0, 0); SB(); k0[1] = LDK(0, 1); SB(); k0[2] = LDK(0, 2);
    SB();
    BAR();
    SB();
#define PF(dst_, t_, j_) do { WAIT_VM(2); dst_ = LDK(t_, j_); } while (0)
    PF(k0[3], 0, 3);
    PF(k1[0], 1, 0); PF(k1[1], 1, 1); PF(k1[2], 1, 2); PF(k1[3], 1, 3);
    PF(k2[0], 2, 0); PF(k2[1], 2, 1); PF(k2[2], 2, 2); PF(k2[3], 2, 3);
    PF(k3[0], 3, 0); PF(k3[1], 3, 1); PF(k3[2], 3, 2); PF(k3[3], 3, 3);
    PF(W[0], 4, 0); PF(W[1], 4, 1); PF(W[2], 4, 2); PF(W[3], 4, 3);
    PF(W[4], 5, 0); PF(W[5], 5, 1); PF(W[6], 5, 2); PF(W[7], 5, 3);
    _Pragma("unroll 1") for (unsigned it = 0; it < 64u * SPIN_CAP; ++it) { if (__builtin_amdgcn_readfirstlane(ctl2[1])) break; __builtin_amdgcn_s_sleep(1); }
    asm volatile("" ::: "memory");
    epoch = __builtin_amdgcn_readfirstlane(ctl2[2]);
    const v4u qf = *(const LAS v4u*)(lds + L_Q + ((r >> 2) & 1) * 256 + (4 * gq + (r & 3)) * 16);
    const v4u zero4 = {0u, 0u, 0u, 0u};
    v4u B0 = (r < 8) ? qf : zero4, B1 = (r < 8) ? zero4 : qf;
    asm volatile("" : "+v"(B0), "+v"(B1));
    PF(R[0], 6, 0); PF(R[1], 6, 1); PF(R[2], 6, 2);
    LAS float* Sw = (LAS float*)(lds + L_S + wave * 4096);
    const int sidx = gq * 8 + ((r >> 2) & 1) * 4 + (r & 2) + (r >> 3);
    float mx = -INFINITY;
    const v4f z4f = {0.f, 0.f, 0.f, 0.f};
#define K_POST(i_) do { \
      const float keep = (r & 2) ? ((r & 1) ? Db[3] : Db[2]) : ((r & 1) ? Da[1] : Da[0]); \
      const float send = (r & 2) ? ((r & 1) ? Da[3] : Da[2]) : ((r & 1) ? Db[1] : Db[0]); \
      float y = keep + dpp<XOR2>(send); y += dpp<XOR1>(y); \
      const float sc = rbf(rbf(y) * 0.08838834764831845f); \
      mx = fmaxf(mx, sc); \
      if ((r & 1) == 0) Sw[(i_) * 32 + sidx] = sc; } while (0)
#define STEP4(OP0, OP1, OP2, OP3, LC, LN) do { \
      OP0; SB(); R[3] = LC(3); SB(); OP1; SB(); R[0] = LN(0); SB(); OP2; SB(); R[1] = LN(1); SB(); OP3; SB(); R[2] = LN(2); SB(); } while (0)
#define STEP4B(OP0, OP1, OP2, OP3, LN) do { \
      OP0; SB(); R[0] = LN(0); SB(); OP1; SB(); R[1] = LN(1); SB(); OP2; SB(); R[2] = LN(2); SB(); OP3; SB(); R[3] = LN(3); SB(); } while (0)
#define STEP4C(OP0, OP1, OP2, OP3, LN) do { \
      OP0; SB(); OP1; SB(); R[0] = LN(0); SB(); OP2; SB(); R[1] = LN(1); SB(); OP3; SB(); R[2] = LN(2); SB(); } while (0)
#define K_TILE(t_, LC, LN) do { v4f Da, Db; \
      STEP4(Da = mfma(R[0], B0, z4f), Da = mfma(R[1], B1, Da), Db = mfma(R[2], B0, z4f), Db = mfma(R[3], B1, Db), LC, LN); \
      K_POST(t_); } while (0)
#define K_PRE(t_, X) do { v4f Da = mfma(X[0], B0, z4f); Da = mfma(X[1], B1, Da); v4f Db = mfma(X[2], B0, z4f); Db = mfma(X[3], B1, Db); K_POST(t_); } while (0)
#define LC_(j) LDK(t__, j)
#define LN_(j) LDK(t__ + 1, j)
#define KT(t_) do { const int t__ = (t_); K_TILE(t__, LC_, LN_); } while (0)
    KT(6); K_PRE(0, k0);
    KT(7); K_PRE(1, k1);
    KT(8); K_PRE(2, k2);
    KT(9); K_PRE(3, k3);
    KT(10); K_PRE(4, (W + 0));
    KT(11); K_PRE(5, (W + 4));
    KT(12);
    KT(13);
    unsigned sched = (tstart + (unsigned)CU_NOM) * 16u;
    int budget = LCAP;
#define LATE() (budget > 0 && (int)((unsigned)__builtin_amdgcn_s_memrealtime() * 16u - sched) > 0)
#define HOLD() do { while ((int)((unsigned)__builtin_amdgcn_s_memrealtime() * 16u - sched) < -(EARLYTH) * 16) __builtin_amdgcn_s_sleep(1); } while (0)
    KT(14);
#pragma unroll 1
    for (int t0 = 15; t0 < 31; t0 += 4) {
      int t = t0;
      HOLD();
      if (LATE()) {
        R[3] = LDK(t0, 3); SB();
#pragma unroll 1
        for (int t__ = t0; t__ < t0 + 3; ++t__) { v4f Da, Db;
          STEP4B(Da = mfma(R[0], B0, z4f), Da = mfma(R[1], B1, Da), Db = mfma(R[2], B0, z4f), Db = mfma(R[3], B1, Db), LN_); K_POST(t__); }
        { const int t__ = t0 + 3; v4f Da, Db;
          STEP4C(Da = mfma(R[0], B0, z4f), Da = mfma(R[1], B1, Da), Db = mfma(R[2], B0, z4f), Db = mfma(R[3], B1, Db), LN_); K_POST(t__); }
        t = t0 + 4; --budget;
      }
      asm volatile("" : "+s"(t));
#pragma unroll 1
      for (; t < t0 + 4; ++t) KT(t);
      sched += 4u * (unsigned)TAUN;
    }
    {
#undef LN_
#define LN_(j) LDV(vbase, j)
      KT(31);
    }
    asm volatile("" ::: "memory");
    mx = fmaxf(mx, dpp<XOR1>(mx)); mx = fmaxf(mx, dpp<XOR2>(mx)); mx = fmaxf(mx, dpp<XOR8>(mx)); mx = x32max(x16max(mx));
    const float m0 = __builtin_bit_cast(float, __builtin_amdgcn_readlane((int)asu(mx), 0)), m1 = __builtin_bit_cast(float, __builtin_amdgcn_readlane((int)asu(mx), 4));
    {
      const float mh = (lane & 1) ? m1 : m0;
      float l = 0.f;
      v4f sv[4];
#pragma unroll
      for (int k = 0; k < 4; ++k) sv[k] = *(const LAS v4f*)((LAS unsigned char*)Sw + (lane + 64 * k) * 16);
#pragma unroll
      for (int k = 0; k < 4; ++k) {
        v4u pk;
#pragma unroll
        for (int e = 0; e < 4; ++e) { const float p = __builtin_amdgcn_exp2f((sv[k][e] - mh) * LOG2E); l += p; pk[e] = cvtpk(p, p); }
        *(LAS v4u*)((LAS unsigned char*)Sw + (lane + 64 * k) * 16) = pk;
      }
      l += dpp<XOR2>(l); l += dpp<ROR4>(l); l += dpp<XOR8>(l); l = x32sum(x16sum(l));
      const float l0 = __builtin_bit_cast(float, __builtin_amdgcn_readlane((int)asu(l), 0)), l1 = __builtin_bit_cast(float, __builtin_amdgcn_readlane((int)asu(l), 1));
      if (lane == 0) *(LAS v4f*)(lds + L_ML + wave * 16) = v4f{m0, l0, m1, l1};
    }
    asm volatile("" ::: "memory");
    unsigned msk[4];
#pragma unroll
    for (int w = 0; w < 4; ++w) msk[w] = ((r >> 2) == w) ? ((r & 1) ? 0xffff0000u : 0x0000ffffu) : 0u;
    const unsigned pql = (unsigned)(gq * 8 + ((r >> 1) & 1) * 4) * 4;
    const LAS unsigned char* pbase = (const LAS unsigned char*)Sw + pql;
    v4f acc = z4f;
    v4u pq = *(const LAS v4u*)(pbase);
#define AOP(l_) v4u{pq[l_] & msk[0], pq[l_] & msk[1], pq[l_] & msk[2], pq[l_] & msk[3]}
#define PVM(A_, R_) do { acc = mfma(A_, R_, acc); asm volatile("" : "+v"(acc)); } while (0)
#define LCV_(j) LDV(cur__, j)
#define LNV_(j) LDV(nxt__, j)
#define V_TILE(cur_, nxt_, pn_) do { \
      const v4u A0 = AOP(0), A1 = AOP(1), A2 = AOP(2), A3 = AOP(3); \
      pq = *(const LAS v4u*)(pbase + (pn_) * 128); SB(); \
      const unsigned cur__ = (cur_), nxt__ = (nxt_); (void)cur__; \
      STEP4(PVM(A0, R[0]), PVM(A1, R[1]), PVM(A2, R[2]), PVM(A3, R[3]), LCV_, LNV_); } while (0)
#define VOWN(n_) (vbase + (unsigned)(n_) * 32768u)
    V_TILE(VOWN(0), VOWN(1), 1);
    TAIL_CONSTS();
    wpa = wo + (size_t)(512 * xcd + 64 * tw + 8 * (lane >> 3)) * D + 64 * (is_wo ? wcg : 0) + 8 * (lane & 7);
#define W_LOADS() do { _Pragma("unroll") for (int i = 0; i < 8; ++i) W[i] = ldnt(wpa + i * D); SB(); } while (0)
#define V_TILEX(STEP, nxt_, pn_) do { \
      const v4u A0 = AOP(0), A1 = AOP(1), A2 = AOP(2), A3 = AOP(3); \
      pq = *(const LAS v4u*)(pbase + (pn_) * 128); SB(); \
      const unsigned nxt__ = (nxt_); \
      STEP(PVM(A0, R[0]), PVM(A1, R[1]), PVM(A2, R[2]), PVM(A3, R[3]), LNV_); } while (0)
    V_TILE(VOWN(1), VOWN(2), 2);
    V_TILE(VOWN(2), VOWN(3), 3);
    sched += 4u * (unsigned)TAUN + (unsigned)VOFFS * 16u;
#pragma unroll 1
    for (int n0 = 3; n0 < 31; n0 += 4) {
      if (tt == 0 && n0 >= GMLT) ctl2[3] = (unsigned)n0;
      int m = n0;
      HOLD();
      if (LATE()) {
        R[3] = LDV(VOWN(n0), 3); SB();
#pragma unroll 1
        for (int n = n0; n < n0 + 3; ++n) { V_TILEX(STEP4B, VOWN(n + 1), n + 1); }
        V_TILEX(STEP4C, VOWN(n0 + 4), n0 + 4);
        m = n0 + 4; --budget;
      }
      asm volatile("" : "+s"(m));
#pragma unroll 1
      for (; m < n0 + 4; ++m) { V_TILE(VOWN(m), VOWN(m + 1), m + 1); }
      sched += 4u * (unsigned)TAUN;
    }
    V_TILE(VOWN(31), 0x7f000000u, 0);
    if (is_wo) W_LOADS();
    LAS float* oa = (LAS float*)(lds + L_OACC + wave * 1024);
    *(LAS v2f*)(oa + 8 * r + 2 * gq) = v2f{acc[0], acc[1]};
    *(LAS v2f*)(oa + 128 + 8 * r + 2 * gq) = v2f{acc[2], acc[3]};
  } else {
    const unsigned ev = __builtin_amdgcn_raw_buffer_load_b32(rW, 0, WS_E, AUX_SC1);
    SB();
    BAR();
    SB();
    epoch = __builtin_amdgcn_readfirstlane(ev) + 1u;
    const unsigned tg = epoch & 0xfffu;
    _Pragma("unroll 1") for (unsigned it = 0; it < 64u * SPIN_CAP; ++it) { if (__builtin_amdgcn_readfirstlane(ctl2[0]) >= 3u) break; }
    asm volatile("" ::: "memory");
    const unsigned qt = epoch << 16;
    {
      float v = 0.f;
#pragma unroll
      for (int w = 0; w < 3; ++w) v += ((const LAS float*)(lds + L_QP))[w * 16 + r];
      const unsigned gr = qt | (cvtpk(v, v) & 0xffffu);
      const v4u g4 = {dppu<0x00>(gr), dppu<0x55>(gr), dppu<0xAA>(gr), dppu<0xFF>(gr)};
      if ((lane & 51) == 0) __builtin_amdgcn_raw_buffer_store_b128(g4, rW, WS_QG + (unsigned)(16 * cb + lane) * 4, 0, AUX_SC1);
    }
    const unsigned o0 = WS_QG + (unsigned)g * 1024u + (unsigned)lane * 16u;
    v4u pa;
    _Pragma("unroll 1") for (int i = 0; i < QLAZY; ++i) __builtin_amdgcn_s_sleep(16);
    _Pragma("unroll 1") for (unsigned it = 0; it < SPIN_CAP; ++it) {
      pa = poll128(rW, o0, 0);
      if (__all((((pa[0] ^ qt) | (pa[1] ^ qt) | (pa[2] ^ qt) | (pa[3] ^ qt)) >> 16) == 0u)) break;
      __builtin_amdgcn_s_sleep(8);
    }
    *(LAS v2u*)(lds + L_Q + lane * 8) = v2u{__builtin_amdgcn_perm(pa[1], pa[0], 0x05040100u), __builtin_amdgcn_perm(pa[3], pa[2], 0x05040100u)};
    if (lane == 0) ctl2[2] = epoch;
    asm volatile("s_waitcnt lgkmcnt(0)" ::: "memory");
    if (lane == 0) ctl2[1] = 1u;
    v4f ml;
    _Pragma("unroll 1") for (unsigned it = 0; it < 64u * SPIN_CAP; ++it) {
      ml = *(const volatile LAS v4f*)(lds + L_ML + (lane & 7) * 16);
      if (__all(asu(ml[1]) != 0u && asu(ml[3]) != 0u)) break;
      __builtin_amdgcn_s_sleep(16);
    }
    float M0 = ml[0], M1 = ml[2];
    M0 = fmaxf(M0, dpp<XOR1>(M0)); M0 = fmaxf(M0, dpp<XOR2>(M0)); M0 = fmaxf(M0, dpp<XOR7>(M0));
    M1 = fmaxf(M1, dpp<XOR1>(M1)); M1 = fmaxf(M1, dpp<XOR2>(M1)); M1 = fmaxf(M1, dpp<XOR7>(M1));
    const float f0 = __builtin_amdgcn_exp2f((ml[0] - M0) * LOG2E), f1 = __builtin_amdgcn_exp2f((ml[2] - M1) * LOG2E);
    float L0 = ml[1] * f0, L1 = ml[3] * f1;
    L0 += dpp<XOR1>(L0); L0 += dpp<XOR2>(L0); L0 += dpp<XOR7>(L0);
    L1 += dpp<XOR1>(L1); L1 += dpp<XOR2>(L1); L1 += dpp<XOR7>(L1);
    _Pragma("unroll 1") for (unsigned it = 0; it < 64u * SPIN_CAP; ++it) { if (__builtin_amdgcn_readfirstlane(ctl2[3])) break; __builtin_amdgcn_s_sleep(16); }
    if (lane == 0) __builtin_amdgcn_raw_buffer_store_b128(v4u{(asu(M0) & 0xfffff000u) | tg, enc(L0, tg), (asu(M1) & 0xfffff000u) | tg, enc(L1, tg)}, rW, WS_ML + (unsigned)b * 16, 0, AUX_SC1);
    v4u gm;
    _Pragma("unroll 1") for (unsigned it = 0; it < 64u * SPIN_CAP; ++it) { if (__builtin_amdgcn_readfirstlane(ctl2[3]) >= (unsigned)HWT) break; __builtin_amdgcn_s_sleep(8); }
    _Pragma("unroll 1") for (unsigned it = 0; it < SPIN_CAP; ++it) {
      gm = poll128(rW, WS_ML + (unsigned)(16 * g + r) * 16, 0);
      if (__all(bad4(gm, tg) == 0u)) break;
      __builtin_amdgcn_s_sleep(8);
    }
    {
      const v4f gv = dec4(gm);
      const float G0 = row16max(gv[0]), G1 = row16max(gv[2]);
      const float T0 = row16sum(gv[1] * __builtin_amdgcn_exp2f((gv[0] - G0) * LOG2E)), T1 = row16sum(gv[3] * __builtin_amdgcn_exp2f((gv[2] - G1) * LOG2E));
      if (lane < 8) *(LAS v2f*)(lds + L_F + lane * 8) = v2f{__builtin_amdgcn_exp2f((ml[0] - G0) * LOG2E) / T0, __builtin_amdgcn_exp2f((ml[2] - G1) * LOG2E) / T1};
    }
    TAIL_CONSTS();
  }
  const unsigned tg = epoch & 0xfffu;
  BAR();
#define PISSUE(off_, d_) asm volatile("global_load_dwordx4 %0, %2, %3 sc1\n\tglobal_load_dwordx4 %1, %2, %4 sc1" : "+v"(qa_), "+v"(qb_) : "v"(off_), "s"(ws), "s"(ws + (d_)) : "memory")
#define POLL2(RES_, off_, d_) \
      v4u qa_ = {0u, 0u, 0u, 0u}, qb_ = {0u, 0u, 0u, 0u}; \
      PISSUE(off_, d_); __builtin_amdgcn_s_sleep(PPS); \
      _Pragma("unroll 1") for (unsigned it = 0; it < SPIN_CAP; ++it) { \
        PISSUE(off_, d_); \
        asm volatile("s_waitcnt vmcnt(2)" : "+v"(qa_), "+v"(qb_) :: "memory"); \
        if (__all((bad4(qa_, tg) | bad4(qb_, tg)) == 0u)) break; \
      } \
      RES_ = dec4(qa_) + dec4(qb_);
  if (tw < 8) {
    const float f = *(const LAS float*)(lds + c_mf);
    v4f o = f * *(const LAS v4f*)(lds + c_mo);
#pragma unroll
    for (int e = 0; e < 4; ++e) { float t = o[e]; t += dpp<XOR8>(t); o[e] = x32sum(x16sum(t)); }
    if (lane < 8) __builtin_amdgcn_raw_buffer_store_b128(mark4(o, tg), rW, c_po, 0, AUX_SC1);
  }
  if (is_wo) {
    if (tw < 8) {
      W_PAIR();
      const unsigned off = c_g;
      v4u p0, p1, p2, p3;
      _Pragma("unroll 1") for (unsigned it = 0; it < SPIN_CAP; ++it) {
        p0 = poll128(rW, off, 0); p1 = poll128(rW, off, 512); p2 = poll128(rW, off, 1024); p3 = poll128(rW, off, 1536);
        if (__all((bad4(p0, tg) | bad4(p1, tg) | bad4(p2, tg) | bad4(p3, tg)) == 0u)) break;
      }
      const v4f o = (dec4(p0) + dec4(p1)) + (dec4(p2) + dec4(p3));
      const bool b1 = (lane & 2) != 0;
      float u0 = (b1 ? o[2] : o[0]) + dpp<XOR2>(b1 ? o[0] : o[2]), u1 = (b1 ? o[3] : o[1]) + dpp<XOR2>(b1 ? o[1] : o[3]);
      u0 += dpp<XOR3>(dpp<XOR7>(u0)); u1 += dpp<XOR3>(dpp<XOR7>(u1));
      const unsigned pk = cvtpk(u0, u1);
      const unsigned a0 = dppu<0x00>(pk), a1 = dppu<0xAA>(pk), a2 = dppu<0x55>(pk), a3 = dppu<0xFF>(pk);
      float a[8];
#pragma unroll
      for (int k = 0; k < 4; ++k) {
        a[2 * k] = dot2(W[6][k], a3, dot2(W[4][k], a2, dot2(W[2][k], a1, dot2(W[0][k], a0, 0.f))));
        a[2 * k + 1] = dot2(W[7][k], a3, dot2(W[5][k], a2, dot2(W[3][k], a1, dot2(W[1][k], a0, 0.f))));
      }
      const float t = tr8(a, lane);
      *(LAS float*)(lds + c_l1) = t;
    }
    BAR();
    if (tw < 2) {
      v4f y = *(const LAS v4f*)(lds + c_l2);
#pragma unroll
      for (int e = 0; e < 4; ++e) { float t = y[e]; t += dpp<XOR8>(t); y[e] = x32sum(x16sum(t)); }
      if (lane < 8) __builtin_amdgcn_raw_buffer_store_b128(mark4(y, tg), rW, c_so, 0, AUX_SC1);
    }
  } else if (is_fin) {
    const int ec = ((lane >> 4) & 1) * 2 + (lane >> 5);
    unsigned off = WS_Z + (unsigned)(tt >> 3) * 2304 + fj * 128 + (tt & 7) * 16;
    unsigned l3 = L_R4 + (tw * 32 + (lane & 7) * 4 + ec) * 4, lp = L_R4 + ((lane >> 5) * 160 + (lane & 31)) * 4, oo = (unsigned)(fj * 32 + lane) * 2;
    float cb2 = bflo(((const GAS u16*)b2)[fj * 32 + (lane & 31)]);
    if (tw == 8) {
      const float xv = bflo(((const GAS u16*)x)[fj * 32 + (lane & 7) * 4 + ec]);
      v4u yq;
      _Pragma("unroll 1") for (unsigned it = 0; it < SPIN_CAP; ++it) {
        yq = poll128(rW, WS_Y + (unsigned)(lane >> 3) * 2304 + fj * 128 + (lane & 7) * 16, 0);
        if (__all(bad4(yq, tg) == 0u)) break;
        __builtin_amdgcn_s_sleep(2);
      }
      v4f y = dec4(yq);
#pragma unroll
      for (int e = 0; e < 4; ++e) y[e] += dpp<XOR8>(y[e]);
      ((LAS float*)(lds + L_H))[(lane & 7) * 4 + ec] = rbf(xv + rbf(x32tr(x16tr(y[0], y[2]), x16tr(y[1], y[3]))));
    }
    BAR();
    float hres = ((const LAS float*)(lds + L_H))[lane & 31];
    asm volatile("" : "+v"(hres), "+v"(lp), "+v"(oo), "+v"(off), "+v"(l3), "+v"(cb2));
    __builtin_amdgcn_s_sleep(FSL);
    v4f zf;
    POLL2(zf, off, 72 * 2304)
    float t = x32tr(x16tr(zf[0], zf[2]), x16tr(zf[1], zf[3]));
    t += dpp<XOR8>(t);
    *(LAS float*)(lds + l3) = t;
    BAR();
    if (tw == 0) {
      const LAS float* pp = (const LAS float*)(lds + lp);
      const float v = x32sum(((pp[0] + pp[32]) + (pp[64] + pp[96])) + pp[128]);
      const float o = hres + rbf(rbf(v) + cb2);
      if (lane < 32) *(GAS u16*)((GAS unsigned char*)out + oo) = (u16)cvtpk(o, o);
      if (fj == 0 && lane == 0) __builtin_amdgcn_raw_buffer_store_b32(epoch, rW, 0, WS_E, AUX_SC1);
    }
    asm volatile("" :: "v"(qa_), "v"(qb_));
  }
}

extern "C" void kernel_launch(void* const* d_in, const int* in_sizes, int n_in, void* d_out, int out_size, void* d_ws, size_t ws_size, hipStream_t stream) {
  hipLaunchKernelGGL(fused_decode, dim3(256), dim3(NTHR), 0, stream,
                     (const u16*)d_in[0], (const u16*)d_in[1], (const u16*)d_in[2], (const u16*)d_in[3], (const u16*)d_in[4],
                     (const u16*)d_in[6], (const u16*)d_in[7], (const u16*)d_in[8], (const u16*)d_in[9], (u16*)d_out, (unsigned char*)d_ws);
}
```
